```python
import torch

def _softmax(x, axis=-1):
    return torch.softmax(x, dim=axis)

def _gelu(x):
    return torch.nn.functional.gelu(x, approximate='tanh')

def _silu(x):
    return torch.nn.functional.silu(x)

def _sigmoid(x):
    return torch.sigmoid(x)

def _layernorm(x, axis=-1, eps=1e-05):
    m = x.mean(dim=axis, keepdim=True)
    v = x.var(dim=axis, keepdim=True, unbiased=False)
    return (x - m) / torch.sqrt(v + eps)

def _scatter_add(tgt, idx, src, axis=0):
    return tgt.clone().index_add_(axis, idx, src)

def _segment_sum(src, seg, n):
    out = torch.zeros((n,) + src.shape[1:], dtype=src.dtype)
    return out.index_add_(0, seg, src)

def _pad(x, pw):
    flat = tuple((v for pair in reversed(pw) for v in pair))
    return torch.nn.functional.pad(x, flat)
kernel_name = 'sequential_preLN__linear_attn_block__mixer_gmlp__prompt'

def setup_inputs():

    _w = lambda a: (lambda t: t.to(torch.bfloat16) if t.is_floating_point() else t)(torch.as_tensor(a))

    def _A_setup():
        B, S, D = (1, 2048, 448)
        return {'x': _w((lambda *s: torch.randn(*s, dtype=torch.bfloat16) * 0.05)(B, S, D)), 'wqkv': _w((lambda *s: torch.randn(*s, dtype=torch.bfloat16) * 0.1)(D, 3 * D)), 'wo': _w((lambda *s: torch.randn(*s, dtype=torch.bfloat16) * 0.05)(D, D))}
    _A_in = _A_setup()

    def _B_setup():
        B, S, D, F = (1, 2048, 448, 1792)
        Fh = F // 2
        return {'x': _w((lambda *s: torch.randn(*s, dtype=torch.bfloat16) * 0.05)(B, S, D)), 'w_in': _w((lambda *s: torch.randn(*s, dtype=torch.bfloat16) * 0.2)(D, 2 * Fh)), 'w_sp': _w(0.01 * (lambda *s: torch.randn(*s, dtype=torch.bfloat16) * 0.2)(S, S)), 'b_sp': _w(1.0 + 0.01 * (lambda *s: torch.randn(*s, dtype=torch.bfloat16) * 0.05)(S)), 'w_out': _w((lambda *s: torch.randn(*s, dtype=torch.bfloat16) * 0.05)(Fh, D))}
    _B_in = _B_setup()
    return {'A_x': _A_in['x'], 'A_wqkv': _A_in['wqkv'], 'A_wo': _A_in['wo'], 'B_x': _B_in['x'], 'B_w_in': _B_in['w_in'], 'B_w_sp': _B_in['w_sp'], 'B_b_sp': _B_in['b_sp'], 'B_w_out': _B_in['w_out']}

def reference(A_x, A_wqkv, A_wo, B_x, B_w_in, B_w_sp, B_b_sp, B_w_out):

    def _A_ref(x, wqkv, wo):
        B, S, D = x.shape
        H = 16
        Dh = D // H
        qkv = (lambda x, *d: x.permute(*d))((x @ wqkv).reshape(B, S, 3, H, Dh), 2, 0, 3, 1, 4)
        q, k, v = (qkv[0], qkv[1], qkv[2])
        phi = lambda t: torch.where(t > 0, t + 1, torch.exp(t)) + 1e-06
        pq, pk = (phi(q), phi(k))
        kv = torch.einsum('bhsd,bhse->bhde', pk, v)
        z = torch.einsum('bhsd->bhd', pk)
        num = torch.einsum('bhqd,bhde->bhqe', pq, kv)
        den = torch.einsum('bhqd,bhd->bhq', pq, z)[..., None] + 1e-06
        o = num / den
        return (lambda x, *d: x.permute(*d))(o, 0, 2, 1, 3).reshape(B, S, D) @ wo

    def _B_ref(x, w_in, w_sp, b_sp, w_out):
        B, S, D = x.shape
        F = w_in.shape[-1]
        Fh = F // 2
        z = _gelu(torch.einsum('bsd,df->bsf', x, w_in))
        z1 = z[..., :Fh].clone()
        z2 = _layernorm(z[..., Fh:], axis=-1)
        g = torch.einsum('ts,bsf->btf', w_sp, z2) + b_sp[None, :, None]
        return torch.einsum('bsf,fd->bsd', z1 * g, w_out)
    x = A_x
    h = x + _A_ref(_layernorm(x, axis=-1), A_wqkv, A_wo)
    h2 = h + _B_ref(_layernorm(h, axis=-1), B_w_in, B_w_sp, B_b_sp, B_w_out)
    return h2
```

```cpp
#include <hip/hip_runtime.h>
#include <hip/hip_bf16.h>
#include <stdint.h>
namespace naive {
constexpr int S = 2048, D = 448, H = 16, DH = 28, F = 1792, FH = 896;
__device__ __forceinline__ float bf2f(unsigned short b) { return __uint_as_float(((unsigned)b) << 16); }
__device__ __forceinline__ unsigned short f2bf(float f) { unsigned u = __float_as_uint(f); u += 0x7FFFu + ((u >> 16) & 1u); return (unsigned short)(u >> 16); }

template <bool INBF>
__global__ void ln_kernel(const void* in, int ld_in, int off, int n, float* out, int ld_out) {
  int row = blockIdx.x; __shared__ float red[256];
  float s = 0;
  for (int j = threadIdx.x; j < n; j += 256) { float v = INBF ? bf2f(((const unsigned short*)in)[row * ld_in + off + j]) : ((const float*)in)[row * ld_in + off + j]; s += v; }
  red[threadIdx.x] = s; __syncthreads();
  for (int o = 128; o > 0; o >>= 1) { if (threadIdx.x < o) red[threadIdx.x] += red[threadIdx.x + o]; __syncthreads(); }
  float m = red[0] / n; __syncthreads();
  s = 0;
  for (int j = threadIdx.x; j < n; j += 256) { float v = INBF ? bf2f(((const unsigned short*)in)[row * ld_in + off + j]) : ((const float*)in)[row * ld_in + off + j]; s += (v - m) * (v - m); }
  red[threadIdx.x] = s; __syncthreads();
  for (int o = 128; o > 0; o >>= 1) { if (threadIdx.x < o) red[threadIdx.x] += red[threadIdx.x + o]; __syncthreads(); }
  float r = 1.0f / sqrtf(red[0] / n + 1e-5f);
  for (int j = threadIdx.x; j < n; j += 256) { float v = INBF ? bf2f(((const unsigned short*)in)[row * ld_in + off + j]) : ((const float*)in)[row * ld_in + off + j]; out[row * ld_out + j] = (v - m) * r; }
}
template <bool ABF, bool BBF>
__global__ void gemm_kernel(const void* A, int lda, const void* B, int ldb, float* C, int ldc, int M, int N, int K) {
  __shared__ float sa[16][17], sb[16][17];
  int tx = threadIdx.x & 15, ty = threadIdx.x >> 4;
  int row = blockIdx.y * 16 + ty, col = blockIdx.x * 16 + tx;
  float acc = 0;
  for (int k0 = 0; k0 < K; k0 += 16) {
    sa[ty][tx] = ABF ? bf2f(((const unsigned short*)A)[(size_t)row * lda + k0 + tx]) : ((const float*)A)[(size_t)row * lda + k0 + tx];
    sb[ty][tx] = BBF ? bf2f(((const unsigned short*)B)[(size_t)(k0 + ty) * ldb + col]) : ((const float*)B)[(size_t)(k0 + ty) * ldb + col];
    __syncthreads();
    for (int k = 0; k < 16; ++k) acc += sa[ty][k] * sb[k][tx];
    __syncthreads();
  }
  C[(size_t)row * ldc + col] = acc;
}
__device__ __forceinline__ float phi(float t) { return (t > 0.f ? t + 1.f : expf(t)) + 1e-6f; }
__global__ void kv_kernel(const float* qkv, float* kv) {
  int h = blockIdx.x / DH, d = blockIdx.x % DH; int e = threadIdx.x;
  if (e > DH) return;
  float acc = 0;
  for (int s = 0; s < S; ++s) { float pk = phi(qkv[s * 3 * D + D + h * DH + d]); float v = e < DH ? qkv[s * 3 * D + 2 * D + h * DH + e] : 1.0f; acc += pk * v; }
  kv[(h * DH + d) * 32 + e] = acc;
}
__global__ void o_kernel(const float* qkv, const float* kv, float* o) {
  int s = blockIdx.x; int j = threadIdx.x; if (j >= D) return;
  int h = j / DH, e = j % DH; float num = 0, den = 0;
  for (int d = 0; d < DH; ++d) { float pq = phi(qkv[s * 3 * D + h * DH + d]); num += pq * kv[(h * DH + d) * 32 + e]; den += pq * kv[(h * DH + d) * 32 + DH]; }
  o[s * D + j] = num / (den + 1e-6f);
}
__global__ void add_x_kernel(const unsigned short* x, const float* a, float* h) { int i = blockIdx.x * 256 + threadIdx.x; h[i] = bf2f(x[i]) + a[i]; }
__global__ void gelu_kernel(float* z) { int i = blockIdx.x * 256 + threadIdx.x; float v = z[i]; z[i] = 0.5f * v * (1.f + tanhf(0.7978845608028654f * (v + 0.044715f * v * v * v))); }
__global__ void y_kernel(const float* z, const float* g, const unsigned short* b, float* y) { int i = blockIdx.x * 256 + threadIdx.x; int t = i / FH, f = i % FH; y[i] = z[t * F + f] * (g[i] + bf2f(b[t])); }
__global__ void fin_kernel(const float* h, const float* a, unsigned short* out) { int i = blockIdx.x * 256 + threadIdx.x; out[i] = f2bf(h[i] + a[i]); }

struct Bufs { float *ln1, *qkv, *kv, *o, *attn, *h, *ln2, *z, *z2n, *g, *y, *mo; };
inline Bufs carve(void* ws) {
  float* p = (float*)ws; Bufs b;
  b.ln1 = p; p += S * D; b.qkv = p; p += S * 3 * D; b.kv = p; p += H * DH * 32; b.o = p; p += S * D; b.attn = p; p += S * D; b.h = p; p += S * D;
  b.ln2 = p; p += S * D; b.z = p; p += S * F; b.z2n = p; p += S * FH; b.g = p; p += S * FH; b.y = p; p += S * FH; b.mo = p; p += S * D;
  return b;
}
inline void run(void* const* d_in, void* d_out, void* ws, hipStream_t st) {
  const unsigned short* x = (const unsigned short*)d_in[0]; const unsigned short* wqkv = (const unsigned short*)d_in[1]; const unsigned short* wo = (const unsigned short*)d_in[2];
  const unsigned short* w_in = (const unsigned short*)d_in[4]; const unsigned short* w_sp = (const unsigned short*)d_in[5]; const unsigned short* b_sp = (const unsigned short*)d_in[6]; const unsigned short* w_out = (const unsigned short*)d_in[7];
  Bufs b = carve(ws);
  ln_kernel<true><<<S, 256, 0, st>>>(x, D, 0, D, b.ln1, D);
  gemm_kernel<false, true><<<dim3(3 * D / 16, S / 16), 256, 0, st>>>(b.ln1, D, wqkv, 3 * D, b.qkv, 3 * D, S, 3 * D, D);
  kv_kernel<<<H * DH, 32, 0, st>>>(b.qkv, b.kv);
  o_kernel<<<S, 448, 0, st>>>(b.qkv, b.kv, b.o);
  gemm_kernel<false, true><<<dim3(D / 16, S / 16), 256, 0, st>>>(b.o, D, wo, D, b.attn, D, S, D, D);
  add_x_kernel<<<S * D / 256, 256, 0, st>>>(x, b.attn, b.h);
  ln_kernel<false><<<S, 256, 0, st>>>(b.h, D, 0, D, b.ln2, D);
  gemm_kernel<false, true><<<dim3(F / 16, S / 16), 256, 0, st>>>(b.ln2, D, w_in, F, b.z, F, S, F, D);
  gelu_kernel<<<S * F / 256, 256, 0, st>>>(b.z);
  ln_kernel<false><<<S, 256, 0, st>>>(b.z, F, FH, FH, b.z2n, FH);
  gemm_kernel<true, false><<<dim3(FH / 16, S / 16), 256, 0, st>>>(w_sp, S, b.z2n, FH, b.g, FH, S, FH, S);
  y_kernel<<<S * FH / 256, 256, 0, st>>>(b.z, b.g, b_sp, b.y);
  gemm_kernel<false, true><<<dim3(D / 16, S / 16), 256, 0, st>>>(b.y, FH, w_out, D, b.mo, D, S, D, FH);
  fin_kernel<<<S * D / 256, 256, 0, st>>>(b.h, b.mo, (unsigned short*)d_out);
}
}
extern "C" void kernel_launch(void* const* d_in, const int* in_sizes, int n_in, void* d_out, int out_size, void* d_ws, size_t ws_size, hipStream_t stream) {
  naive::run(d_in, d_out, d_ws, stream);
}
```

```cpp
#include <hip/hip_runtime.h>
#include <hip/hip_bf16.h>
#include <stdint.h>
#include <type_traits>
#pragma clang diagnostic ignored "-Wint-to-pointer-cast"

namespace {

#define DI __device__ __forceinline__
#define LAS __attribute__((address_space(3)))
#define GAS __attribute__((address_space(1)))
typedef unsigned u32;
typedef unsigned long long u64;
typedef u32 u32x2 __attribute__((ext_vector_type(2)));
typedef u32 u32x4 __attribute__((ext_vector_type(4)));
typedef float f32x2 __attribute__((ext_vector_type(2)));
typedef float f32x4 __attribute__((ext_vector_type(4)));
typedef float f32x16 __attribute__((ext_vector_type(16)));
typedef __bf16 bf16x8 __attribute__((ext_vector_type(8)));
typedef __bf16 bf16x2 __attribute__((ext_vector_type(2)));
typedef short s16x4 __attribute__((ext_vector_type(4)));
#define RLX_AGENT __ATOMIC_RELAXED, __HIP_MEMORY_SCOPE_AGENT

constexpr u32 WS_EPOCH = 0, WS_TMO = 128;
constexpr u32 WS_FLAGS = 4096;
constexpr u32 F_KVP = 0, F_KVR = 1, F_PQ = 2, F_H = 3, F_ZS = 4, F_Z = 5, F_Y = 6, F_CS = 7;
constexpr u32 WS_KVP = 1u << 20;
constexpr u32 WS_KVR = WS_KVP + (2u << 20);
constexpr u32 WS_CS = WS_KVR + 32768;
constexpr u32 WS_HST = WS_CS + 8192;
constexpr u32 WS_ZST = WS_HST + 16 * 7 * 128 * 8;
constexpr u32 WS_PQ = 4u << 20;
constexpr u32 WS_H = 6u << 20;
constexpr u32 WS_Z = 8u << 20;
constexpr u32 WS_Y = 12u << 20;
constexpr u32 WS_DBG = 16u << 20;
constexpr u32 WS_BYTES = 17u << 20;
static_assert(WS_ZST + 16 * 14 * 128 * 8 <= WS_PQ, "ws map");

constexpr u32 L_X = 0, XSTR = 912;
constexpr u32 L_WKV = 64 * XSTR;
constexpr u32 L_WQ = L_WKV;
constexpr u32 L_KST = L_WKV + 53248;
constexpr u32 L_WO = 0;
constexpr u32 L_WZ2 = 57344;
constexpr u32 L_WZ1 = 0;
constexpr u32 L_KVR = 114688;
constexpr u32 L_ZR = 0;
constexpr u32 L_SPA = 114688, L_SPB = 65536;
constexpr u32 L_WOUT = 0;
constexpr u32 L_MISC = 160768;
constexpr u32 LDS_BYTES = 163840;

constexpr u32 SPIN_CAP = 1u << 16;
constexpr float LOG2E = 1.4426950408889634f;

DI u32 rfl(u32 x) { return __builtin_amdgcn_readfirstlane(x); }
DI u32x4 make_srd(const void* p, u32 bytes) {
  const u64 a = (u64)p;
  return u32x4{(u32)a, (u32)(a >> 32) & 0xffffu, bytes, 0x00020000u};
}
DI u32 cvtpk(float lo, float hi) {
  f32x2 v = {lo, hi};
  return __builtin_bit_cast(u32, __builtin_convertvector(v, bf16x2));
}
DI float asf(u32 x) { return __builtin_bit_cast(float, x); }
DI u32 asu(float x) { return __builtin_bit_cast(u32, x); }
DI float bflo(u32 x) { return __builtin_bit_cast(float, x << 16); }
DI float bfhi(u32 x) { return __builtin_bit_cast(float, x & 0xffff0000u); }
DI f32x16 mfma32(u32x4 a, u32x4 b, f32x16 c) {
  return __builtin_amdgcn_mfma_f32_32x32x16_bf16(__builtin_bit_cast(bf16x8, a), __builtin_bit_cast(bf16x8, b), c, 0, 0, 0);
}
DI f32x4 mfma16(u32x4 a, u32x4 b, f32x4 c) {
  return __builtin_amdgcn_mfma_f32_16x16x32_bf16(__builtin_bit_cast(bf16x8, a), __builtin_bit_cast(bf16x8, b), c, 0, 0, 0);
}
DI u32x2 tr64(u32 a) { return __builtin_bit_cast(u32x2, __builtin_amdgcn_ds_read_tr16_b64_v4i16((LAS s16x4*)a)); }
DI u32x4 trfrag(u32 a0, u32 a1) {
  const u32x2 lo = tr64(a0), hi = tr64(a1);
  return u32x4{lo.x, lo.y, hi.x, hi.y};
}
DI u32x4 lds128(u32 a) { return *(LAS u32x4*)a; }
DI f32x4 lds128f(u32 a) { return *(LAS f32x4*)a; }
DI void bar() { asm volatile("s_waitcnt lgkmcnt(0)\n\ts_barrier" ::: "memory"); }
template <int N> DI void waitvm() { asm volatile("s_waitcnt vmcnt(%0)" ::"i"(N) : "memory"); }
template <int IMM, bool SC1> DI void dma(u32 ldsaddr, u32 voff, u32x4 srd, u32 soff) {
  if constexpr (SC1)
    asm volatile("s_mov_b32 m0, %0\n\ts_nop 0\n\tbuffer_load_dwordx4 %1, %2, %3 offen offset:%4 sc1 lds" ::"s"(ldsaddr), "v"(voff), "s"(srd), "s"(soff), "i"(IMM) : "memory");
  else
    asm volatile("s_mov_b32 m0, %0\n\ts_nop 0\n\tbuffer_load_dwordx4 %1, %2, %3 offen offset:%4 lds" ::"s"(ldsaddr), "v"(voff), "s"(srd), "s"(soff), "i"(IMM) : "memory");
}
template <int IMM, bool SC1> DI void ld128(u32x4& d, u32 voff, u32x4 srd, u32 soff) {
  if constexpr (SC1) asm volatile("buffer_load_dwordx4 %0, %1, %2, %3 offen offset:%4 sc1" : "=v"(d) : "v"(voff), "s"(srd), "s"(soff), "i"(IMM) : "memory");
  else asm volatile("buffer_load_dwordx4 %0, %1, %2, %3 offen offset:%4" : "=v"(d) : "v"(voff), "s"(srd), "s"(soff), "i"(IMM) : "memory");
}
template <int IMM, bool SC1> DI void ld64(u32x2& d, u32 voff, u32x4 srd, u32 soff) {
  if constexpr (SC1) asm volatile("buffer_load_dwordx2 %0, %1, %2, %3 offen offset:%4 sc1" : "=v"(d) : "v"(voff), "s"(srd), "s"(soff), "i"(IMM) : "memory");
  else asm volatile("buffer_load_dwordx2 %0, %1, %2, %3 offen offset:%4" : "=v"(d) : "v"(voff), "s"(srd), "s"(soff), "i"(IMM) : "memory");
}
template <int N, typename T> DI void waitpin(T& a) { asm volatile("s_waitcnt vmcnt(%1)" : "+v"(a) : "i"(N) : "memory"); }
template <typename T> DI void pin(T& a) { asm volatile("" : "+v"(a)); }
template <int OFF, bool ACC> DI void take4(u32x4& r0, u32x4& r1, u32x4& r2, u32x4& r3, u32 a0, u32 a1, u32 a2, u32 a3) {
  if constexpr (ACC)
    asm volatile("ds_read_b128 %0, %4 offset:%8\n\tds_read_b128 %1, %5 offset:%8\n\tds_read_b128 %2, %6 offset:%8\n\tds_read_b128 %3, %7 offset:%8\n\ts_waitcnt lgkmcnt(0)"
                 : "=&a"(r0), "=&a"(r1), "=&a"(r2), "=&a"(r3) : "v"(a0), "v"(a1), "v"(a2), "v"(a3), "i"(OFF) : "memory");
  else
    asm volatile("ds_read_b128 %0, %4 offset:%8\n\tds_read_b128 %1, %5 offset:%8\n\tds_read_b128 %2, %6 offset:%8\n\tds_read_b128 %3, %7 offset:%8\n\ts_waitcnt lgkmcnt(0)"
                 : "=&v"(r0), "=&v"(r1), "=&v"(r2), "=&v"(r3) : "v"(a0), "v"(a1), "v"(a2), "v"(a3), "i"(OFF) : "memory");
}
DI void st128(__amdgpu_buffer_rsrc_t rs, u32 off, u32x4 v) { __builtin_amdgcn_raw_buffer_store_b128(v, rs, off, 0, 16); }
DI void st64(__amdgpu_buffer_rsrc_t rs, u32 off, u32x2 v) { __builtin_amdgcn_raw_buffer_store_b64(v, rs, off, 0, 16); }
template <int CTRL> DI float dpp_add(float x) {
  return x + __builtin_bit_cast(float, __builtin_amdgcn_update_dpp(0, __builtin_bit_cast(int, x), CTRL, 0xf, 0xf, true));
}
DI float add_x16(float x) {
  const auto r = __builtin_amdgcn_permlane16_swap(__builtin_bit_cast(u32, x), __builtin_bit_cast(u32, x), false, false);
  return __builtin_bit_cast(float, (u32)r[0]) + __builtin_bit_cast(float, (u32)r[1]);
}
DI float add_x32(float x) {
  const auto r = __builtin_amdgcn_permlane32_swap(__builtin_bit_cast(u32, x), __builtin_bit_cast(u32, x), false, false);
  return __builtin_bit_cast(float, (u32)r[0]) + __builtin_bit_cast(float, (u32)r[1]);
}
DI float phi(float t) { return (t > 0.f ? t + 1.0f : __builtin_amdgcn_exp2f(t * LOG2E)) + 1e-6f; }
DI f32x2 gelu2(f32x2 x) {
  const float c1 = -2.0f * 0.7978845608028654f * LOG2E, c3 = c1 * 0.044715f;
  const f32x2 p = __builtin_elementwise_fma(x * x, f32x2{c3, c3}, f32x2{c1, c1}) * x;
  const f32x2 e = f32x2{__builtin_amdgcn_exp2f(p.x), __builtin_amdgcn_exp2f(p.y)} + f32x2{1.f, 1.f};
  return x * f32x2{__builtin_amdgcn_rcpf(e.x), __builtin_amdgcn_rcpf(e.y)};
}

DI GAS u64* flag_ptr(char* ws, u32 arr, u32 idx) { return (GAS u64*)(ws + WS_FLAGS + (arr * 256 + idx) * 128); }
DI void set_flag(char* ws, u32 arr, u32 idx, u64 tag) { __hip_atomic_store(flag_ptr(ws, arr, idx), tag, RLX_AGENT); }
DI void poll1(char* ws, GAS u64* p, bool on, u64 tag) {
  for (u32 spins = 0;; ++spins) {
    u64 v = tag;
    if (on) v = __hip_atomic_load(p, RLX_AGENT);
    if (__all(v == tag)) break;
    if ((spins & 63u) == 63u) {
      if (spins > SPIN_CAP) __hip_atomic_store((GAS u64*)(ws + WS_TMO), tag, RLX_AGENT);
      if (__hip_atomic_load((GAS u64*)(ws + WS_TMO), RLX_AGENT) == tag) break;
    }
  }
  __builtin_amdgcn_fence(__ATOMIC_ACQUIRE, "wavefront");
}
DI void poll4(char* ws, GAS u64* p, u64 tag) {
  for (u32 spins = 0;; ++spins) {
    const u64 a = __hip_atomic_load(p, RLX_AGENT), b = __hip_atomic_load(p + 64 * 16, RLX_AGENT), c = __hip_atomic_load(p + 128 * 16, RLX_AGENT),
              d = __hip_atomic_load(p + 192 * 16, RLX_AGENT);
    if (__all(a == tag && b == tag && c == tag && d == tag)) break;
    if ((spins & 63u) == 63u) {
      if (spins > SPIN_CAP) __hip_atomic_store((GAS u64*)(ws + WS_TMO), tag, RLX_AGENT);
      if (__hip_atomic_load((GAS u64*)(ws + WS_TMO), RLX_AGENT) == tag) break;
    }
  }
  __builtin_amdgcn_fence(__ATOMIC_ACQUIRE, "wavefront");
}

#define STAMP(i)
#define TSOUT()

template <int I, int N, typename F> DI void sfor(F&& f) {
  if constexpr (I < N) {
    f(std::integral_constant<int, I>{});
    sfor<I + 1, N>(f);
  }
}
#define SFOR(i, a, b) sfor<a, b>([&](auto i##_c) __attribute__((always_inline)) { constexpr int i = decltype(i##_c)::value;
#define SEND });

constexpr int NRES = 10, NSTREAM = 16 - NRES;
constexpr u32 sp_slot(int s) { return s < 2 ? L_SPA + s * 4096 : L_SPB + (s - 2) * 4096; }

__global__ void __launch_bounds__(256, 1)
fused_kernel(const void* __restrict__ x_, const void* __restrict__ wqkv_, const void* __restrict__ wo_, const void* __restrict__ win_,
             const void* __restrict__ wsp_, const void* __restrict__ bsp_, const void* __restrict__ wout_, void* __restrict__ out_, char* __restrict__ ws) {
  __shared__ __attribute__((aligned(1024))) char smem[LDS_BYTES];
  asm volatile("" ::"v"((u32)(uintptr_t)(LAS char*)smem));
  const u32 tid = threadIdx.x, l = tid & 63, w = rfl(tid >> 6), wg = blockIdx.x;
  const u32 xcd = wg & 7, rr_ = wg >> 3;
  const u32 p1 = 8 * (xcd >> 1) + (rr_ & 7), j = 4 * (xcd & 1) + (rr_ >> 3);
  const u32 id = p1 * 8 + j;
  const u32 P = p1 >> 1, g14 = 2 * j + (p1 & 1);
  STAMP(0);
  const u32x4 sX = make_srd(x_, 2048 * 896), sWQKV = make_srd(wqkv_, 448 * 2688), sWS = make_srd(ws, WS_BYTES);
  const __amdgpu_buffer_rsrc_t rWS = __builtin_amdgcn_make_buffer_rsrc(ws, 0, WS_BYTES, 0x00020000);
  const u32 zero = 0;

  u32x2 epoch;
  ld64<0, true>(epoch, zero, sWS, WS_EPOCH);
  u32x4 xr[2][7];
  {
    const u32 vo = (64 * p1 + 16 * w + (l >> 3)) * 896 + (l & 7) * 16;
    SFOR(i, 0, 7) ld128<i * 128, false>(xr[0][i], vo, sX, zero); SEND
    SFOR(i, 0, 7) ld128<i * 128, false>(xr[1][i], vo, sX, 8 * 896); SEND
  }
  {
    u32 vkv[7];
    SFOR(m, 0, 7)
      const u32 c = 64 * w + l + 256 * m, row = c / 14, pos = c - 14 * row;
      vkv[m] = row * 2688 + 896 + 16 * pos + (pos >= 7 ? 784 : 0) + 112 * j;
    SEND
    SFOR(m, 0, 25) dma<0, false>(L_WKV + (w + 4 * m) * 1024, vkv[m % 7], sWQKV, (m / 7) * 128 * 2688); SEND
  }
  waitpin<39>(epoch);
  const u64 tag = (((u64)rfl(epoch.y) << 32) | rfl(epoch.x)) + 1;
  SFOR(r, 0, 2)
    waitvm<25 + (1 - r) * 7>();
    SFOR(i, 0, 7) pin(xr[r][i]); SEND
    f32x2 v[28];
    SFOR(i, 0, 7) SFOR(e, 0, 4) v[4 * i + e] = f32x2{bflo(xr[r][i][e]), bfhi(xr[r][i][e])}; SEND SEND
    f32x2 s2 = v[0];
    SFOR(i, 1, 28) s2 += v[i]; SEND
    float s = s2.x + s2.y;
    s = dpp_add<0xB1>(s); s = dpp_add<0x4E>(s); s = dpp_add<0x141>(s);
    const float mean = s * (1.0f / 448);
    f32x2 q2 = {0.f, 0.f};
    SFOR(i, 0, 28) v[i] -= f32x2{mean, mean}; q2 = __builtin_elementwise_fma(v[i], v[i], q2); SEND
    float q = q2.x + q2.y;
    q = dpp_add<0xB1>(q); q = dpp_add<0x4E>(q); q = dpp_add<0x141>(q);
    const float rs = __builtin_amdgcn_rsqf(q * (1.0f / 448) + 1e-5f);
    const u32 a = L_X + (16 * w + 8 * r + (l >> 3)) * XSTR + (l & 7) * 16;
    SFOR(i, 0, 7)
      u32x4 o;
      SFOR(e, 0, 4) const f32x2 t = v[4 * i + e] * f32x2{rs, rs}; o[e] = cvtpk(t.x, t.y); SEND
      *(LAS u32x4*)(a + i * 128) = o;
    SEND
  SEND
  STAMP(1);
  const u32 hh = w & 1, th = w >> 1, hf = l >> 5;
  const u32 aX = L_X + (32 * th + (l & 31)) * XSTR + hf * 16;
  const u32 trq = (l >> 2) & 3, trp = l & 3, blk = (l >> 4) & 1;
  {
    f32x16 ak = {}, av = {};
    const u32 aW = L_WKV + (8 * hf + trq) * 224 + 56 * hh + 32 * blk + 8 * trp;
    u32 qc = 64 * (w - 1) + l, qrow = qc / 7, qpos = qc - 7 * qrow;
    u32 vq = qrow * 2688 + 16 * qpos + 112 * j;
    SFOR(s, 0, 7)
      constexpr int cnt[7] = {4, 7, 11, 14, 18, 21, 25}, qb[8] = {0, 0, 4, 9, 14, 17, 17, 17};
      if (w == 0) waitvm<25 - cnt[s]>(); else waitvm<25 - cnt[s] + qb[s]>();
      bar();
      if (w != 0) {
        SFOR(m, qb[s], qb[s + 1])
          dma<0, false>(L_WQ + (w - 1 + 3 * m) * 1024, vq, sWQKV, zero);
          qpos += 3;
          const bool wrap = qpos >= 7;
          vq += wrap ? 28 * 2688 + 48 - 112 : 27 * 2688 + 48;
          qpos -= wrap ? 7 : 0;
        SEND
      }
      SFOR(kk, 0, 4)
        constexpr int ks = 4 * s + kk;
        const u32x4 xa = lds128(aX + ks * 32);
        const u32x4 bk = trfrag(aW + (16 * ks) * 224, aW + (16 * ks + 4) * 224);
        const u32x4 bv = trfrag(aW + (16 * ks) * 224 + 112, aW + (16 * ks + 4) * 224 + 112);
        ak = mfma32(xa, bk, ak);
        av = mfma32(xa, bv, av);
      SEND
    SEND
    STAMP(2);
    const u32 col = l & 31;
    u32x4 pk[2], pv[2];
    SFOR(i, 0, 8)
      const u32 a = cvtpk(phi(ak[2 * i]), phi(ak[2 * i + 1])), b = cvtpk(av[2 * i], av[2 * i + 1]);
      pk[i >> 2][i & 3] = col < 28 ? a : 0u;
      pv[i >> 2][i & 3] = col < 28 ? b : (col == 28 ? 0x3f803f80u : 0u);
    SEND
    f32x16 kv = {};
    kv = mfma32(pv[0], pk[0], kv);
    kv = mfma32(pv[1], pk[1], kv);
    const u32 a = L_KST + w * 4096 + hf * 512 + col * 4;
    SFOR(i, 0, 16) *(LAS float*)(a + ((i & 3) + 8 * (i >> 2)) * 128) = kv[i]; SEND
  }
  bar();
  if (w == 0) {
    const u32 quad = l & 7;
    SFOR(i, 0, 8)
      constexpr int hd = i >> 2;
      const u32 rho = 8 * (i & 3) + (l >> 3);
      const u32 e = rho < 16 ? rho : ((rho & 3) == 3 ? 28u : 16 + 3 * ((rho - 16) >> 2) + (rho & 3));
      const u32 a = L_KST + hd * 4096 + e * 128 + quad * 16;
      const f32x4 v = lds128f(a) + lds128f(a + 8192);
      st128(rWS, WS_KVP + ((j * 32 + (rho & ~1u) + hd) * 32 + p1) * 256 + (rho & 1) * 128 + quad * 16, __builtin_bit_cast(u32x4, v));
    SEND
    waitvm<0>();
    if (l == 0) set_flag(ws, F_KVP, id, tag);
  } else {
    waitvm<0>();
  }
  STAMP(3);
  bar();
  {
    f32x16 aq = {};
    const u32 aW = L_WQ + (8 * hf + trq) * 112 + 56 * hh + 32 * blk + 8 * trp;
    SFOR(ks, 0, 28)
      const u32x4 xa = lds128(aX + ks * 32);
      const u32x4 wq = trfrag(aW + (16 * ks) * 112, aW + (16 * ks + 4) * 112);
      aq = mfma32(wq, xa, aq);
    SEND
    u32x4 pq[2];
    SFOR(i, 0, 8)
      const u32 a = cvtpk(phi(aq[2 * i]), phi(aq[2 * i + 1]));
      pq[i >> 2][i & 3] = (i >= 6 && hf) ? 0u : a;
    SEND
    const u32 o = WS_PQ + ((p1 * 4 + 2 * th + blk) * 16 + 2 * j + hh) * 1024 + hf * 256 + (l & 15) * 16;
    st128(rWS, o, pq[0]);
    st128(rWS, o + 512, pq[1]);
  }
  waitvm<0>();
  bar();
  if (tid == 0) set_flag(ws, F_PQ, id, tag);
  STAMP(4);
  const u32x4 sWIN = make_srd(win_, 448 * 3584);
  const u32 vrow8 = l >> 3, vpos = l & 7;
  if (w != 0) {
    if (j < 7) {
      const u32x4 sWO = make_srd(wo_, 448 * 896);
      const u32 vo = vrow8 * 896 + (vpos ^ (((l >> 4) & 3) << 1)) * 16 + 128 * j;
      for (u32 n = w - 1; n < 56; n += 3) dma<0, false>(L_WO + n * 1024, vo, sWO, n * 8 * 896);
      const u32 vz = vrow8 * 3584 + (vpos ^ (((l >> 4) & 1) << 2)) * 16 + 128 * g14 + 1792;
      for (u32 n = w - 1; n < 56; n += 3) dma<0, false>(L_WZ2 + n * 1024, vz, sWIN, n * 8 * 3584);
    }
  } else {
    poll1(ws, flag_ptr(ws, F_KVP, (l & 31) * 8 + j), l < 32, tag);
    STAMP(5);
    u32x4 pr[8];
    const u32 vo = WS_KVP + (j * 32 + p1) * 8192 + l * 16;
    SFOR(i, 0, 4) ld128<i * 1024, true>(pr[i], vo, sWS, zero); SEND
    SFOR(i, 0, 4) ld128<i * 1024, true>(pr[4 + i], vo, sWS, 4096); SEND
    waitvm<0>();
    SFOR(i, 0, 8) pin(pr[i]); SEND
    f32x4 s = __builtin_bit_cast(f32x4, pr[0]);
    SFOR(i, 1, 8) s += __builtin_bit_cast(f32x4, pr[i]); SEND
    SFOR(e, 0, 4) s[e] = add_x32(add_x16(s[e])); SEND
    const u32 a = cvtpk(s[0], s[1]), b = cvtpk(s[2], s[3]);
    const u32 a2 = __builtin_amdgcn_update_dpp(0, (int)a, 0x4E, 0xf, 0xf, true), b2 = __builtin_amdgcn_update_dpp(0, (int)b, 0x4E, 0xf, 0xf, true);
    const u32 qd = l & 7, g = 2 * (qd >> 2) + (qd & 1), i = p1 >> 1, rq = (i & 7) >> 1;
    const u32 pos = 2 * (g ^ ((4 - rq) & 3)) + ((l >> 3) & 1);
    if (l < 16 && !(qd & 2)) st128(rWS, WS_KVR + ((2 * j + (p1 & 1)) * 16 + i) * 128 + pos * 16, u32x4{a, b, a2, b2});
    waitvm<0>();
    if (l == 0) set_flag(ws, F_KVR, id, tag);
    STAMP(6);
  }
  if (j == 7) {
    if (p1 < 28) {
      u32x4 wr[14];
      const u32 vo = (112 * w + vrow8) * 3584 + vpos * 16 + 128 * p1;
      SFOR(i, 0, 14) ld128<0, false>(wr[i], vo, sWIN, i * 8 * 3584); SEND
      waitvm<0>();
      f32x2 c[4] = {};
      SFOR(i, 0, 14) pin(wr[i]); SFOR(e, 0, 4) c[e] += f32x2{bflo(wr[i][e]), bfhi(wr[i][e])}; SEND SEND
      float cs[8];
      SFOR(e, 0, 8) cs[e] = add_x32(add_x16(dpp_add<0x128>(c[e >> 1][e & 1]))); SEND
      if (l < 8) {
        *(LAS f32x4*)(L_MISC + w * 256 + l * 32) = f32x4{cs[0], cs[1], cs[2], cs[3]};
        *(LAS f32x4*)(L_MISC + w * 256 + l * 32 + 16) = f32x4{cs[4], cs[5], cs[6], cs[7]};
      }
      bar();
      if (w == 0) {
        if (l < 16) {
          const u32 a = L_MISC + l * 16;
          const f32x4 t = (lds128f(a) + lds128f(a + 256)) + (lds128f(a + 512) + lds128f(a + 768));
          st128(rWS, WS_CS + p1 * 256 + l * 16, __builtin_bit_cast(u32x4, t));
        }
        waitvm<0>();
        if (l == 0) set_flag(ws, F_CS, p1, tag);
      }
    }
    TSOUT();
    return;
  }
  if (w == 0) {
    poll1(ws, flag_ptr(ws, F_PQ, p1 * 8 + (l & 7)), l < 8, tag);
    poll4(ws, flag_ptr(ws, F_KVR, l), tag);
    if (wg == 0 && l == 0) __hip_atomic_store((GAS u64*)(ws + WS_EPOCH), tag, RLX_AGENT);
  }
  bar();
  STAMP(7);
  const u32 t16 = l & 15, g4 = l >> 4;
  f32x4 hacc[4];
  {
    u32x4 kr[8], pq[16];
    u32x2 xt[4];
    const u32 vk = WS_KVR + w * 8192 + l * 16;
    SFOR(i, 0, 4) ld128<i * 1024, true>(kr[i], vk, sWS, zero); SEND
    SFOR(i, 0, 4) ld128<i * 1024, true>(kr[4 + i], vk, sWS, 4096); SEND
    const u32 vp = WS_PQ + (p1 * 4 + w) * 16384 + l * 16;
    SFOR(i, 0, 16) ld128<(i & 3) * 1024, true>(pq[i], vp, sWS, (i >> 2) * 4096); SEND
    const u32 vx = (64 * p1 + 16 * w + t16) * 896 + 128 * j + 8 * g4;
    SFOR(i, 0, 4) ld64<i * 32, false>(xt[i], vx, sX, zero); SEND
    waitvm<20>();
    SFOR(i, 0, 8) pin(kr[i]); *(LAS u32x4*)(L_KVR + w * 8192 + i * 1024 + l * 16) = kr[i]; SEND
    bar();
    STAMP(8);
    const u32 aK = L_KVR + (t16 >> 1) * 128 + (2 * (g4 ^ ((4 - (t16 >> 2)) & 3)) + (t16 & 1)) * 16;
    u32 aO[2][4];
    {
      const u32 r0 = 4 * g4 + trq, r1 = 16 + 3 * g4 + (trq < 2 ? trq : 2);
      SFOR(y, 0, 4)
        aO[0][y] = L_WO + r0 * 128 + ((y ^ ((r0 >> 1) & 3)) * 32) + 8 * trp;
        aO[1][y] = L_WO + r1 * 128 + ((y ^ ((r1 >> 1) & 3)) * 32) + 8 * trp;
      SEND
    }
    SFOR(n, 0, 4) hacc[n] = f32x4{0.f, 0.f, 0.f, 0.f}; SEND
    SFOR(h, 0, 16)
      waitpin<19 - h>(pq[h]);
      const f32x4 z4 = {0.f, 0.f, 0.f, 0.f};
      const f32x4 d0 = mfma16(lds128(aK + h * 2048), pq[h], z4), d1 = mfma16(lds128(aK + h * 2048 + 1024), pq[h], z4);
      const float r = __builtin_amdgcn_rcpf(d1[3] + 1e-6f);
      const u32x4 ob = {cvtpk(d0[0] * r, d0[1] * r), cvtpk(d0[2] * r, d0[3] * r), cvtpk(d1[0] * r, d1[1] * r), cvtpk(d1[2] * r, 0.f)};
      SFOR(n, 0, 4)
        constexpr int y = n ^ (2 * (h & 1));
        hacc[n] = mfma16(trfrag(aO[0][y] + h * 3584, aO[1][y] + h * 3584), ob, hacc[n]);
      SEND
    SEND
    waitvm<0>();
    float s = 0.f, q = 0.f;
    SFOR(n, 0, 4)
      pin(xt[n]);
      hacc[n] += f32x4{bflo(xt[n].x), bfhi(xt[n].x), bflo(xt[n].y), bfhi(xt[n].y)};
      SFOR(e, 0, 4) s += hacc[n][e]; q = __builtin_fmaf(hacc[n][e], hacc[n][e], q); SEND
    SEND
    const u32 o = WS_H + ((2 * p1 + (w >> 1)) * 28 + 4 * j + (g4 >> 1)) * 1024 + (g4 & 1) * 512 + (16 * (w & 1) + t16) * 16;
    SFOR(m, 0, 2)
      st128(rWS, o + m * 2048, u32x4{cvtpk(hacc[2 * m][0], hacc[2 * m][1]), cvtpk(hacc[2 * m][2], hacc[2 * m][3]), cvtpk(hacc[2 * m + 1][0], hacc[2 * m + 1][1]),
                                     cvtpk(hacc[2 * m + 1][2], hacc[2 * m + 1][3])});
    SEND
    s = add_x32(add_x16(s));
    q = add_x32(add_x16(q));
    if (l < 16) st64(rWS, WS_HST + ((P * 7 + j) * 128 + 64 * (p1 & 1) + 16 * w + l) * 8, u32x2{asu(s), asu(q)});
  }
  waitvm<0>();
  bar();
  if (tid == 0) set_flag(ws, F_H, id, tag);
  STAMP(9);
  if (w != 0) {
    const u32 vz = vrow8 * 3584 + (vpos ^ (((l >> 4) & 1) << 2)) * 16 + 128 * g14;
    for (u32 n = w - 1; n < 56; n += 3) dma<0, false>(L_WZ1 + n * 1024, vz, sWIN, n * 8 * 3584);
  } else {
    const u32 m = l < 14 ? l : 0, pp = 2 * P + (m >= 7), jj = m >= 7 ? m - 7 : m;
    GAS u64* fp = l < 14 ? flag_ptr(ws, F_H, pp * 8 + jj) : flag_ptr(ws, F_CS, g14 + (l & 1) * 14);
    poll1(ws, fp, l < 16, tag);
  }
  bar();
  STAMP(10);
  const u32 t32 = l & 31;
  const u32x4 sWSP = make_srd(wsp_, 2048 * 4096);
  u32 vS[2];
  SFOR(i, 0, 2) vS[i] = (128 * P + 32 * w + vrow8) * 4096 + (vpos ^ (4 * i + (l >> 4))) * 16; SEND
  const u32 spA = w * 8192, spB = w * 12288;
  auto sp_issue = [&](auto c_, auto s_) __attribute__((always_inline)) {
    constexpr int c = decltype(c_)::value, s = decltype(s_)::value;
    const u32 kb = ((P + (c >> 1)) & 15) * 256 + (c & 1) * 128;
    SFOR(i, 0, 4) dma<0, false>(sp_slot(s) + (s < 2 ? spA : spB) + i * 1024, vS[i & 1], sWSP, kb + i * 8 * 4096); SEND
  };
  u32 aS[2][4];
  SFOR(k, 0, 4)
    const u32 b = t32 * 128 + (((2 * k + hf) ^ ((t32 >> 1) & 7)) * 16);
    aS[0][k] = L_SPA + spA + b;
    aS[1][k] = L_SPB + spB + b;
  SEND
  u32x4 res[NRES * 8];
  auto sp_take = [&](auto c_, auto s_) __attribute__((always_inline)) {
    constexpr int c = decltype(c_)::value, s = decltype(s_)::value, o = (s < 2 ? s : s - 2) * 4096, r = (c - 2 * NSTREAM) * 4;
    take4<o, (r < 48)>(res[r], res[r + 1], res[r + 2], res[r + 3], aS[s >= 2][0], aS[s >= 2][1], aS[s >= 2][2], aS[s >= 2][3]);
  };
#define IC(v) std::integral_constant<int, (v)>{}
  u32 z1p[16];
  u32 bbits;
  {
    u32x4 hb[28];
    u32x2 st[4];
    u32x4 cs[8];
    const u32 vst = WS_HST + ((P * 7 + 4 * hf) * 128 + 32 * w + t32) * 8;
    SFOR(i, 0, 3) ld64<i * 1024, true>(st[i], vst, sWS, zero); SEND
    ld64<0, true>(st[3], vst + (hf ? 0 : 3072), sWS, zero);
    const u32 vh = WS_H + (4 * P + w) * 28 * 1024 + l * 16;
    SFOR(i, 0, 28) ld128<(i & 3) * 1024, true>(hb[i], vh, sWS, (i >> 2) * 4096); SEND
    const u32 vc2 = WS_CS + (896 + 64 * g14 + 4 * hf) * 4;
    SFOR(i, 0, 8) ld128<i * 32, true>(cs[i], vc2, sWS, zero); SEND
    {
      const u32x4 sB = make_srd(bsp_, 4096);
      asm volatile("buffer_load_ushort %0, %1, %2, 0 offen" : "=v"(bbits) : "v"((128 * P + 32 * w + t32) * 2), "s"(sB) : "memory");
    }
    const u32 rowp = (4 * hf + trq) * 128 + blk * 32;
    const u32 aZ2[2] = {L_WZ2 + rowp + ((trq >> 1) * 64) + 8 * trp, L_WZ2 + rowp + (((trq >> 1) ^ 1) * 64) + 8 * trp};
    const u32 pp = 8 * (2 * (trp & 1) + (trp >> 1));
    const u32 aZ1[2] = {L_WZ1 + rowp + ((trq >> 1) * 64) + pp, L_WZ1 + rowp + (((trq >> 1) ^ 1) * 64) + pp};
    f32x16 z[2] = {};
    SFOR(k, 0, 28)
      if constexpr ((k & 1) == 0) { waitpin<9 + 26 - k>(hb[k]); pin(hb[k + 1]); }
      constexpr int ro = (32 * (k >> 1) + 8 * (k & 1)) * 128;
      SFOR(f, 0, 2) z[f] = mfma32(trfrag(aZ2[f] + ro, aZ2[f] + ro + 2048), hb[k], z[f]); SEND
    SEND
    waitvm<0>();
    STAMP(11);
    SFOR(i, 0, 4) pin(st[i]); SEND
    SFOR(i, 0, 8) pin(cs[i]); SEND
    pin(bbits);
    float rs, nm;
    {
      float s = asf(st[0].x) + asf(st[1].x) + asf(st[2].x);
      float q = asf(st[0].y) + asf(st[1].y) + asf(st[2].y);
      s += hf ? 0.f : asf(st[3].x);
      q += hf ? 0.f : asf(st[3].y);
      s = add_x32(s) * (1.0f / 448);
      q = add_x32(q) * (1.0f / 448);
      rs = __builtin_amdgcn_rsqf(__builtin_fmaxf(q - s * s, 0.f) + 1e-5f);
      nm = -s * rs;
    }
    f32x2 zz[16];
    {
      f32x2 s2 = {0.f, 0.f}, q2 = {0.f, 0.f};
      SFOR(i, 0, 16)
        constexpr int f = i >> 3, e = (2 * i) & 15;
        const f32x4 c = __builtin_bit_cast(f32x4, cs[i >> 1]);
        const f32x2 u = __builtin_elementwise_fma(f32x2{c[(2 * i) & 3], c[((2 * i) & 3) + 1]}, f32x2{nm, nm}, f32x2{z[f][e], z[f][e + 1]} * f32x2{rs, rs});
        zz[i] = gelu2(u);
        s2 += zz[i];
        q2 = __builtin_elementwise_fma(zz[i], zz[i], q2);
      SEND
      const float s = add_x32(s2.x + s2.y), q = add_x32(q2.x + q2.y);
      if (l < 32) st64(rWS, WS_ZST + ((P * 14 + g14) * 128 + 32 * w + l) * 8, u32x2{asu(s), asu(q)});
    }
    waitvm<0>();
    bar();
    if (tid == 0) set_flag(ws, F_ZS, id, tag);
    STAMP(12);
    const u32 vc1 = WS_CS + (64 * g14 + 8 * hf) * 4;
    SFOR(i, 0, 8) ld128<(i >> 2) * 128 + ((i >> 1) & 1) * 64 + (i & 1) * 16, true>(cs[i], vc1, sWS, zero); SEND
    SFOR(s, 0, 5) sp_issue(IC(2 * NSTREAM + s), IC(s)); SEND
    z[0] = f32x16{};
    z[1] = f32x16{};
    SFOR(k, 0, 28)
      constexpr int ro = (32 * (k >> 1) + 8 * (k & 1)) * 128;
      SFOR(f, 0, 2) z[f] = mfma32(trfrag(aZ1[f] + ro, aZ1[f] + ro + 2048), hb[k], z[f]); SEND
      if constexpr (k >= 8 && (k & 3) == 0) {
        constexpr int s = (k - 8) >> 2;
        waitvm<16>();
        sp_take(IC(2 * NSTREAM + s), IC(s));
        sp_issue(IC(2 * NSTREAM + 5 + s), IC(s));
      }
    SEND
    STAMP(13);
    if (w == 0) {
      const u32 m = l < 14 ? l : 0, pp2 = 2 * P + (m >= 7), jj = m >= 7 ? m - 7 : m;
      poll1(ws, flag_ptr(ws, F_ZS, pp2 * 8 + jj), l < 14, tag);
    }
    bar();
    STAMP(14);
    {
      u32x2 zs[7];
      const u32 vz = WS_ZST + ((P * 14 + 7 * hf) * 128 + 32 * w + t32) * 8;
      SFOR(i, 0, 7) ld64<(i & 3) * 1024, true>(zs[i], vz, sWS, (i >> 2) * 4096); SEND
      waitvm<0>();
      float s = 0.f, q = 0.f;
      SFOR(i, 0, 7) pin(zs[i]); s += asf(zs[i].x); q += asf(zs[i].y); SEND
      s = add_x32(s) * (1.0f / 896);
      q = add_x32(q) * (1.0f / 896);
      const float r2 = __builtin_amdgcn_rsqf(__builtin_fmaxf(q - s * s, 0.f) + 1e-5f), n2 = -s * r2;
      const u32 o = WS_Z + (g14 * 64 + 4 * P + w) * 4096 + hf * 512;
      SFOR(c, 0, 4)
        u32x4 v;
        SFOR(e, 0, 4) const f32x2 t = __builtin_elementwise_fma(zz[4 * c + e], f32x2{r2, r2}, f32x2{n2, n2}); v[e] = cvtpk(t.x, t.y); SEND
        st128(rWS, o + c * 1024 + (t32 ^ ((2 * (c & 1) + hf) << 2)) * 16, v);
      SEND
    }
    waitvm<0>();
    bar();
    if (tid == 0) set_flag(ws, F_Z, id, tag);
    STAMP(15);
    SFOR(s, 0, 5) sp_take(IC(2 * NSTREAM + 5 + s), IC(s)); if constexpr (10 + s < 2 * NRES) sp_issue(IC(2 * NSTREAM + 10 + s), IC(s)); SEND
    SFOR(i, 0, 8) pin(cs[i]); SEND
    SFOR(i, 0, 16)
      constexpr int f = i >> 3, e = (2 * i) & 15;
      const f32x4 c = __builtin_bit_cast(f32x4, cs[i >> 1]);
      const f32x2 u = __builtin_elementwise_fma(f32x2{c[(2 * i) & 3], c[((2 * i) & 3) + 1]}, f32x2{nm, nm}, f32x2{z[f][e], z[f][e + 1]} * f32x2{rs, rs});
      const f32x2 t = gelu2(u);
      z1p[i] = cvtpk(t.x, t.y);
    SEND
  }
  static_assert(NRES == 10, "the schedule below is written for 20 resident chunks");
  SFOR(s, 0, 5) waitvm<16>(); sp_take(IC(2 * NSTREAM + 10 + s), IC(s)); sp_issue(IC(2 * NSTREAM + 15 + s), IC(s)); SEND
  SFOR(s, 0, 5) waitvm<16>(); sp_take(IC(2 * NSTREAM + 15 + s), IC(s)); sp_issue(IC(s), IC(s)); SEND
  STAMP(16);
  if (w == 0) poll1(ws, flag_ptr(ws, F_Z, (2 * (l & 15) + (p1 & 1)) * 8 + j), l < 16, tag);
  bar();
  STAMP(17);
  {
    const u32 vZ = WS_Z + g14 * 64 * 4096 + w * 4096 + l * 16;
    auto z_issue = [&](auto st_) __attribute__((always_inline)) {
      constexpr int st = decltype(st_)::value;
      const u32 so = ((P + st) & 15) * 16384, la = L_ZR + (st & 3) * 16384 + w * 4096;
      SFOR(i, 0, 4) dma<i * 1024, true>(la, vZ, sWS, so); SEND
    };
    SFOR(st, 0, 3) z_issue(IC(st)); SEND
    u32 aZ[4];
    {
      const u32 o3 = 2 * blk + (trp >> 1), b0 = L_ZR + o3 * 512 + (((2 * hf) ^ o3) * 4 + trq) * 16 + (trp & 1) * 8;
      SFOR(v, 0, 4) aZ[v] = b0 ^ (64 * (v & 1)) ^ (256 * (v >> 1)); SEND
    }
    f32x16 acc[2] = {};
    SFOR(st, 0, 16)
      constexpr int spc = (st >= 1 && 2 * (st - 1) + 5 < 2 * NSTREAM ? 1 : 0) + (st >= 1 && 2 * (st - 1) + 6 < 2 * NSTREAM ? 1 : 0)
                        + (st >= 2 && 2 * (st - 2) + 5 < 2 * NSTREAM ? 1 : 0) + (st >= 2 && 2 * (st - 2) + 6 < 2 * NSTREAM ? 1 : 0)
                        + (st >= 3 && 2 * (st - 3) + 5 < 2 * NSTREAM ? 1 : 0) + (st >= 3 && 2 * (st - 3) + 6 < 2 * NSTREAM ? 1 : 0);
      constexpr int zl = (st + 1 < 16 ? 1 : 0) + (st + 2 < 16 ? 1 : 0);
      waitvm<4 * (zl + spc)>();
      bar();
      if constexpr (st + 3 < 16) z_issue(IC(st + 3));
      SFOR(cc, 0, 2)
        constexpr int c = 2 * st + cc;
        u32x4 bs[4];
        if constexpr (st < NSTREAM) {
          constexpr int s = c % 5;
          if constexpr (c >= 5) {
            constexpr int ist = (c - 5) >> 1;
            constexpr int zy = (st - ist) - ((st + 3 < 16) ? 0 : 0);
            constexpr int cy = (c + 4 < 2 * NSTREAM ? 4 : 2 * NSTREAM - 1 - c);
            waitvm<4 * (zy + cy)>();
          }
          SFOR(k, 0, 4) bs[k] = lds128(aS[s >= 2][k] + (s < 2 ? s : s - 2) * 4096); SEND
          if constexpr (c + 5 < 2 * NSTREAM) {
            asm volatile("s_waitcnt lgkmcnt(0)" : "+v"(bs[0]), "+v"(bs[1]), "+v"(bs[2]), "+v"(bs[3])::"memory");
            sp_issue(IC(c + 5), IC(s));
          }
        }
        SFOR(k, 0, 4)
          constexpr int ks = 4 * cc + k, off = (st & 3) * 16384 + (ks >> 1) * 4096;
          SFOR(f, 0, 2)
            const u32x4 a = trfrag(aZ[2 * (ks & 1)] + off + f * 2048, aZ[2 * (ks & 1) + 1] + off + f * 2048);
            if constexpr (st < NSTREAM) acc[f] = mfma32(a, bs[k], acc[f]);
            else acc[f] = mfma32(a, res[(c - 2 * NSTREAM) * 4 + k], acc[f]);
          SEND
        SEND
      SEND
    SEND
    STAMP(18);
    const float b = bflo(bbits);
    const u32 o = WS_Y + ((8 * P + 2 * w + blk) * 28 + 2 * g14) * 1024 + hf * 256 + (l & 15) * 16;
    SFOR(c, 0, 4)
      u32x4 v;
      SFOR(e, 0, 4)
        constexpr int i = 8 * (c & 1) + 2 * e;
        const u32 zp = z1p[4 * c + e];
        v[e] = cvtpk(bflo(zp) * (acc[c >> 1][i] + b), bfhi(zp) * (acc[c >> 1][i + 1] + b));
      SEND
      st128(rWS, o + (c >> 1) * 1024 + (c & 1) * 512, v);
    SEND
  }
  waitvm<0>();
  bar();
  if (tid == 0) set_flag(ws, F_Y, id, tag);
  STAMP(19);
  if (w != 0) {
    const u32x4 sWOUT = make_srd(wout_, 896 * 896);
    const u32 x0 = (l >> 4) & 1;
    const u32 vo0 = vrow8 * 896 + (vpos ^ (x0 << 1)) * 16 + 128 * j, vo1 = vrow8 * 896 + (vpos ^ ((x0 | 2) << 1)) * 16 + 128 * j;
    for (u32 n = w - 1; n < 112; n += 3) dma<0, false>(L_WOUT + n * 1024, (n & 1) ? vo1 : vo0, sWOUT, n * 8 * 896);
  } else {
    const u32 m = l < 14 ? l : 0, pp = 2 * P + (m >= 7), jj = m >= 7 ? m - 7 : m;
    poll1(ws, flag_ptr(ws, F_Y, pp * 8 + jj), l < 14, tag);
  }
  bar();
  STAMP(20);
  {
    u32x4 yb[28];
    const u32 vy = WS_Y + (4 * p1 + w) * 28 * 1024 + l * 16;
    SFOR(i, 0, 28) ld128<(i & 3) * 1024, true>(yb[i], vy, sWS, (i >> 2) * 4096); SEND
    waitvm<28>();
    bar();
    STAMP(21);
    u32 aW[4];
    {
      const u32 xs = (trq >> 1) | ((g4 & 1) << 1);
      SFOR(n, 0, 4) aW[n] = L_WOUT + (8 * g4 + trq) * 128 + ((n ^ xs) * 32) + 8 * trp; SEND
    }
    SFOR(k, 0, 28)
      if constexpr ((k & 1) == 0) { waitpin<26 - k>(yb[k]); pin(yb[k + 1]); }
      SFOR(n, 0, 4) hacc[n] = mfma16(trfrag(aW[n] + k * 4096, aW[n] + k * 4096 + 512), yb[k], hacc[n]); SEND
    SEND
    STAMP(22);
    const __amdgpu_buffer_rsrc_t rO = __builtin_amdgcn_make_buffer_rsrc(out_, 0, 2048 * 896, 0x00020000);
    const u32 o = (64 * p1 + 16 * w + t16) * 896 + 128 * j + (g4 & 1) * 32 + (g4 >> 1) * 16;
    SFOR(m, 0, 2)
      const auto r0 = __builtin_amdgcn_permlane16_swap(cvtpk(hacc[2 * m][0], hacc[2 * m][1]), cvtpk(hacc[2 * m + 1][0], hacc[2 * m + 1][1]), false, false);
      const auto r1 = __builtin_amdgcn_permlane16_swap(cvtpk(hacc[2 * m][2], hacc[2 * m][3]), cvtpk(hacc[2 * m + 1][2], hacc[2 * m + 1][3]), false, false);
      __builtin_amdgcn_raw_buffer_store_b128(u32x4{(u32)r0[0], (u32)r1[0], (u32)r0[1], (u32)r1[1]}, rO, o + m * 64, 0, 0);
    SEND
  }
  STAMP(23);
  TSOUT();
}

}

extern "C" void kernel_launch(void* const* d_in, const int* in_sizes, int n_in, void* d_out, int out_size, void* d_ws, size_t ws_size, hipStream_t stream) {
  fused_kernel<<<256, 256, 0, stream>>>(d_in[0], d_in[1], d_in[2], d_in[4], d_in[5], d_in[6], d_in[7], d_out, (char*)d_ws);
}
```

```cpp
#define TS
#define AMP 1
#include <hip/hip_runtime.h>
#include <hip/hip_bf16.h>
#include <stdint.h>
#include <type_traits>
#pragma clang diagnostic ignored "-Wint-to-pointer-cast"

namespace {

#define DI __device__ __forceinline__
#define LAS __attribute__((address_space(3)))
#define GAS __attribute__((address_space(1)))
typedef unsigned u32;
typedef unsigned long long u64;
typedef u32 u32x2 __attribute__((ext_vector_type(2)));
typedef u32 u32x4 __attribute__((ext_vector_type(4)));
typedef float f32x2 __attribute__((ext_vector_type(2)));
typedef float f32x4 __attribute__((ext_vector_type(4)));
typedef float f32x16 __attribute__((ext_vector_type(16)));
typedef __bf16 bf16x8 __attribute__((ext_vector_type(8)));
typedef __bf16 bf16x2 __attribute__((ext_vector_type(2)));
typedef short s16x4 __attribute__((ext_vector_type(4)));
#define RLX_AGENT __ATOMIC_RELAXED, __HIP_MEMORY_SCOPE_AGENT

constexpr u32 WS_EPOCH = 0, WS_TMO = 128;
constexpr u32 WS_FLAGS = 4096;
constexpr u32 F_KVP = 0, F_KVR = 1, F_PQ = 2, F_H = 3, F_ZS = 4, F_Z = 5, F_Y = 6, F_CS = 7;
constexpr u32 WS_KVP = 1u << 20;
constexpr u32 WS_KVR = WS_KVP + (2u << 20);
constexpr u32 WS_CS = WS_KVR + 32768;
constexpr u32 WS_HST = WS_CS + 8192;
constexpr u32 WS_ZST = WS_HST + 16 * 7 * 128 * 8;
constexpr u32 WS_PQ = 4u << 20;
constexpr u32 WS_H = 6u << 20;
constexpr u32 WS_Z = 8u << 20;
constexpr u32 WS_Y = 12u << 20;
constexpr u32 WS_DBG = 16u << 20;
constexpr u32 WS_BYTES = 17u << 20;
static_assert(WS_ZST + 16 * 14 * 128 * 8 <= WS_PQ, "ws map");

constexpr u32 L_X = 0, XSTR = 912;
constexpr u32 L_WKV = 64 * XSTR;
constexpr u32 L_WQ = L_WKV;
constexpr u32 L_KST = L_WKV + 53248;
constexpr u32 L_WO = 0;
constexpr u32 L_WZ2 = 57344;
constexpr u32 L_WZ1 = 0;
constexpr u32 L_KVR = 114688;
constexpr u32 L_ZR = 0;
constexpr u32 L_SPA = 114688, L_SPB = 65536;
constexpr u32 L_WOUT = 0;
constexpr u32 L_MISC = 160768;
constexpr u32 LDS_BYTES = 163840;

constexpr u32 SPIN_CAP = 1u << 16;
constexpr float LOG2E = 1.4426950408889634f;

DI u32 rfl(u32 x) { return __builtin_amdgcn_readfirstlane(x); }
DI u32x4 make_srd(const void* p, u32 bytes) {
  const u64 a = (u64)p;
  return u32x4{(u32)a, (u32)(a >> 32) & 0xffffu, bytes, 0x00020000u};
}
DI u32 cvtpk(float lo, float hi) {
  f32x2 v = {lo, hi};
  return __builtin_bit_cast(u32, __builtin_convertvector(v, bf16x2));
}
DI float asf(u32 x) { return __builtin_bit_cast(float, x); }
DI u32 asu(float x) { return __builtin_bit_cast(u32, x); }
DI float bflo(u32 x) { return __builtin_bit_cast(float, x << 16); }
DI float bfhi(u32 x) { return __builtin_bit_cast(float, x & 0xffff0000u); }
DI f32x16 mfma32(u32x4 a, u32x4 b, f32x16 c) {
  return __builtin_amdgcn_mfma_f32_32x32x16_bf16(__builtin_bit_cast(bf16x8, a), __builtin_bit_cast(bf16x8, b), c, 0, 0, 0);
}
DI f32x4 mfma16(u32x4 a, u32x4 b, f32x4 c) {
  return __builtin_amdgcn_mfma_f32_16x16x32_bf16(__builtin_bit_cast(bf16x8, a), __builtin_bit_cast(bf16x8, b), c, 0, 0, 0);
}
DI u32x2 tr64(u32 a) { return __builtin_bit_cast(u32x2, __builtin_amdgcn_ds_read_tr16_b64_v4i16((LAS s16x4*)a)); }
DI u32x4 trfrag(u32 a0, u32 a1) {
  const u32x2 lo = tr64(a0), hi = tr64(a1);
  return u32x4{lo.x, lo.y, hi.x, hi.y};
}
DI u32x4 lds128(u32 a) { return *(LAS u32x4*)a; }
DI f32x4 lds128f(u32 a) { return *(LAS f32x4*)a; }
DI void bar() { asm volatile("s_waitcnt lgkmcnt(0)\n\ts_barrier" ::: "memory"); }
template <int N> DI void waitvm() { asm volatile("s_waitcnt vmcnt(%0)" ::"i"(N) : "memory"); }
template <int IMM, bool SC1> DI void dma(u32 ldsaddr, u32 voff, u32x4 srd, u32 soff) {
  if constexpr (SC1)
    asm volatile("s_mov_b32 m0, %0\n\ts_nop 0\n\tbuffer_load_dwordx4 %1, %2, %3 offen offset:%4 sc1 lds" ::"s"(ldsaddr), "v"(voff), "s"(srd), "s"(soff), "i"(IMM) : "memory");
  else
    asm volatile("s_mov_b32 m0, %0\n\ts_nop 0\n\tbuffer_load_dwordx4 %1, %2, %3 offen offset:%4 lds" ::"s"(ldsaddr), "v"(voff), "s"(srd), "s"(soff), "i"(IMM) : "memory");
}
template <int IMM, bool SC1> DI void ld128(u32x4& d, u32 voff, u32x4 srd, u32 soff) {
  if constexpr (SC1) asm volatile("buffer_load_dwordx4 %0, %1, %2, %3 offen offset:%4 sc1" : "=v"(d) : "v"(voff), "s"(srd), "s"(soff), "i"(IMM) : "memory");
  else asm volatile("buffer_load_dwordx4 %0, %1, %2, %3 offen offset:%4" : "=v"(d) : "v"(voff), "s"(srd), "s"(soff), "i"(IMM) : "memory");
}
template <int IMM, bool SC1> DI void ld64(u32x2& d, u32 voff, u32x4 srd, u32 soff) {
  if constexpr (SC1) asm volatile("buffer_load_dwordx2 %0, %1, %2, %3 offen offset:%4 sc1" : "=v"(d) : "v"(voff), "s"(srd), "s"(soff), "i"(IMM) : "memory");
  else asm volatile("buffer_load_dwordx2 %0, %1, %2, %3 offen offset:%4" : "=v"(d) : "v"(voff), "s"(srd), "s"(soff), "i"(IMM) : "memory");
}
template <int N, typename T> DI void waitpin(T& a) { asm volatile("s_waitcnt vmcnt(%1)" : "+v"(a) : "i"(N) : "memory"); }
template <typename T> DI void pin(T& a) { asm volatile("" : "+v"(a)); }
template <int OFF, bool ACC> DI void take4(u32x4& r0, u32x4& r1, u32x4& r2, u32x4& r3, u32 a0, u32 a1, u32 a2, u32 a3) {
  if constexpr (ACC)
    asm volatile("ds_read_b128 %0, %4 offset:%8\n\tds_read_b128 %1, %5 offset:%8\n\tds_read_b128 %2, %6 offset:%8\n\tds_read_b128 %3, %7 offset:%8\n\ts_waitcnt lgkmcnt(0)"
                 : "=&a"(r0), "=&a"(r1), "=&a"(r2), "=&a"(r3) : "v"(a0), "v"(a1), "v"(a2), "v"(a3), "i"(OFF) : "memory");
  else
    asm volatile("ds_read_b128 %0, %4 offset:%8\n\tds_read_b128 %1, %5 offset:%8\n\tds_read_b128 %2, %6 offset:%8\n\tds_read_b128 %3, %7 offset:%8\n\ts_waitcnt lgkmcnt(0)"
                 : "=&v"(r0), "=&v"(r1), "=&v"(r2), "=&v"(r3) : "v"(a0), "v"(a1), "v"(a2), "v"(a3), "i"(OFF) : "memory");
}
DI void st128(__amdgpu_buffer_rsrc_t rs, u32 off, u32x4 v) { __builtin_amdgcn_raw_buffer_store_b128(v, rs, off, 0, 16); }
DI void st64(__amdgpu_buffer_rsrc_t rs, u32 off, u32x2 v) { __builtin_amdgcn_raw_buffer_store_b64(v, rs, off, 0, 16); }
template <int CTRL> DI float dpp_add(float x) {
  return x + __builtin_bit_cast(float, __builtin_amdgcn_update_dpp(0, __builtin_bit_cast(int, x), CTRL, 0xf, 0xf, true));
}
DI float add_x16(float x) {
  const auto r = __builtin_amdgcn_permlane16_swap(__builtin_bit_cast(u32, x), __builtin_bit_cast(u32, x), false, false);
  return __builtin_bit_cast(float, (u32)r[0]) + __builtin_bit_cast(float, (u32)r[1]);
}
DI float add_x32(float x) {
  const auto r = __builtin_amdgcn_permlane32_swap(__builtin_bit_cast(u32, x), __builtin_bit_cast(u32, x), false, false);
  return __builtin_bit_cast(float, (u32)r[0]) + __builtin_bit_cast(float, (u32)r[1]);
}
DI float phi(float t) { return (t > 0.f ? t + 1.0f : __builtin_amdgcn_exp2f(t * LOG2E)) + 1e-6f; }
DI f32x2 gelu2(f32x2 x) {
  const float c1 = -2.0f * 0.7978845608028654f * LOG2E, c3 = c1 * 0.044715f;
  const f32x2 p = __builtin_elementwise_fma(x * x, f32x2{c3, c3}, f32x2{c1, c1}) * x;
  const f32x2 e = f32x2{__builtin_amdgcn_exp2f(p.x), __builtin_amdgcn_exp2f(p.y)} + f32x2{1.f, 1.f};
  return x * f32x2{__builtin_amdgcn_rcpf(e.x), __builtin_amdgcn_rcpf(e.y)};
}

DI GAS u64* flag_ptr(char* ws, u32 arr, u32 idx) { return (GAS u64*)(ws + WS_FLAGS + (arr * 256 + idx) * 128); }
DI void set_flag(char* ws, u32 arr, u32 idx, u64 tag) { __hip_atomic_store(flag_ptr(ws, arr, idx), tag, RLX_AGENT); }
DI void poll1(char* ws, GAS u64* p, bool on, u64 tag) {
  for (u32 spins = 0;; ++spins) {
    u64 v = tag;
    if (on) v = __hip_atomic_load(p, RLX_AGENT);
    if (__all(v == tag)) break;
    if ((spins & 63u) == 63u) {
      if (spins > SPIN_CAP) __hip_atomic_store((GAS u64*)(ws + WS_TMO), tag, RLX_AGENT);
      if (__hip_atomic_load((GAS u64*)(ws + WS_TMO), RLX_AGENT) == tag) break;
    }
  }
  __builtin_amdgcn_fence(__ATOMIC_ACQUIRE, "wavefront");
}
DI void poll4(char* ws, GAS u64* p, u64 tag) {
  for (u32 spins = 0;; ++spins) {
    const u64 a = __hip_atomic_load(p, RLX_AGENT), b = __hip_atomic_load(p + 64 * 16, RLX_AGENT), c = __hip_atomic_load(p + 128 * 16, RLX_AGENT),
              d = __hip_atomic_load(p + 192 * 16, RLX_AGENT);
    if (__all(a == tag && b == tag && c == tag && d == tag)) break;
    if ((spins & 63u) == 63u) {
      if (spins > SPIN_CAP) __hip_atomic_store((GAS u64*)(ws + WS_TMO), tag, RLX_AGENT);
      if (__hip_atomic_load((GAS u64*)(ws + WS_TMO), RLX_AGENT) == tag) break;
    }
  }
  __builtin_amdgcn_fence(__ATOMIC_ACQUIRE, "wavefront");
}

#define STAMP(i) do { __builtin_amdgcn_sched_barrier(0); ts[i] = (u32)__builtin_amdgcn_s_memrealtime(); asm volatile("s_waitcnt lgkmcnt(0)" : "+s"(ts[i])); __builtin_amdgcn_sched_barrier(0); } while (0)
#define TSOUT() do { if (tid == 0) { _Pragma("unroll") for (int i_ = 0; i_ < 24; ++i_) ((u32*)(ws + WS_DBG))[wg * 32 + i_] = ts[i_]; } } while (0)

template <int I, int N, typename F> DI void sfor(F&& f) {
  if constexpr (I < N) {
    f(std::integral_constant<int, I>{});
    sfor<I + 1, N>(f);
  }
}
#define SFOR(i, a, b) sfor<a, b>([&](auto i##_c) __attribute__((always_inline)) { constexpr int i = decltype(i##_c)::value;
#define SEND });

constexpr int NRES = 10, NSTREAM = 16 - NRES;
constexpr u32 sp_slot(int s) { return s < 2 ? L_SPA + s * 4096 : L_SPB + (s - 2) * 4096; }

__global__ void __launch_bounds__(256, 1)
fused_kernel(const void* __restrict__ x_, const void* __restrict__ wqkv_, const void* __restrict__ wo_, const void* __restrict__ win_,
             const void* __restrict__ wsp_, const void* __restrict__ bsp_, const void* __restrict__ wout_, void* __restrict__ out_, char* __restrict__ ws) {
  __shared__ __attribute__((aligned(1024))) char smem[LDS_BYTES];
  asm volatile("" ::"v"((u32)(uintptr_t)(LAS char*)smem));
  const u32 tid = threadIdx.x, l = tid & 63, w = rfl(tid >> 6), wg = blockIdx.x;
  const u32 xcd = wg & 7, rr_ = wg >> 3;
  const u32 p1 = 8 * (xcd >> 1) + (rr_ & 7), j = 4 * (xcd & 1) + (rr_ >> 3);
  const u32 id = p1 * 8 + j;
  const u32 P = p1 >> 1, g14 = 2 * j + (p1 & 1);
  u32 ts[24] = {};
  STAMP(0);
  const u32x4 sX = make_srd(x_, 2048 * 896), sWQKV = make_srd(wqkv_, 448 * 2688), sWS = make_srd(ws, WS_BYTES);
  const __amdgpu_buffer_rsrc_t rWS = __builtin_amdgcn_make_buffer_rsrc(ws, 0, WS_BYTES, 0x00020000);
  const u32 zero = 0;

  u32x2 epoch;
  ld64<0, true>(epoch, zero, sWS, WS_EPOCH);
  u32x4 xr[2][7];
  {
    const u32 vo = (64 * p1 + 16 * w + (l >> 3)) * 896 + (l & 7) * 16;
    SFOR(i, 0, 7) ld128<i * 128, false>(xr[0][i], vo, sX, zero); SEND
    SFOR(i, 0, 7) ld128<i * 128, false>(xr[1][i], vo, sX, 8 * 896); SEND
  }
  {
    u32 vkv[7];
    SFOR(m, 0, 7)
      const u32 c = 64 * w + l + 256 * m, row = c / 14, pos = c - 14 * row;
      vkv[m] = row * 2688 + 896 + 16 * pos + (pos >= 7 ? 784 : 0) + 112 * j;
    SEND
    SFOR(m, 0, 25) dma<0, false>(L_WKV + (w + 4 * m) * 1024, vkv[m % 7], sWQKV, (m / 7) * 128 * 2688); SEND
  }
  waitpin<39>(epoch);
  const u64 tag = (((u64)rfl(epoch.y) << 32) | rfl(epoch.x)) + 1;
  SFOR(r, 0, 2)
    waitvm<25 + (1 - r) * 7>();
    SFOR(i, 0, 7) pin(xr[r][i]); SEND
    f32x2 v[28];
    SFOR(i, 0, 7) SFOR(e, 0, 4) v[4 * i + e] = f32x2{bflo(xr[r][i][e]), bfhi(xr[r][i][e])}; SEND SEND
    f32x2 s2 = v[0];
    SFOR(i, 1, 28) s2 += v[i]; SEND
    float s = s2.x + s2.y;
    s = dpp_add<0xB1>(s); s = dpp_add<0x4E>(s); s = dpp_add<0x141>(s);
    const float mean = s * (1.0f / 448);
    f32x2 q2 = {0.f, 0.f};
    SFOR(i, 0, 28) v[i] -= f32x2{mean, mean}; q2 = __builtin_elementwise_fma(v[i], v[i], q2); SEND
    float q = q2.x + q2.y;
    q = dpp_add<0xB1>(q); q = dpp_add<0x4E>(q); q = dpp_add<0x141>(q);
    const float rs = __builtin_amdgcn_rsqf(q * (1.0f / 448) + 1e-5f);
    const u32 a = L_X + (16 * w + 8 * r + (l >> 3)) * XSTR + (l & 7) * 16;
    SFOR(i, 0, 7)
      u32x4 o;
      SFOR(e, 0, 4) const f32x2 t = v[4 * i + e] * f32x2{rs, rs}; o[e] = cvtpk(t.x, t.y); SEND
      *(LAS u32x4*)(a + i * 128) = o;
    SEND
  SEND
  STAMP(1);
  const u32 hh = w & 1, th = w >> 1, hf = l >> 5;
  const u32 aX = L_X + (32 * th + (l & 31)) * XSTR + hf * 16;
  const u32 trq = (l >> 2) & 3, trp = l & 3, blk = (l >> 4) & 1;
  {
    f32x16 ak = {}, av = {};
    const u32 aW = L_WKV + (8 * hf + trq) * 224 + 56 * hh + 32 * blk + 8 * trp;
    u32 qc = 64 * (w - 1) + l, qrow = qc / 7, qpos = qc - 7 * qrow;
    u32 vq = qrow * 2688 + 16 * qpos + 112 * j;
    SFOR(s, 0, 7)
      constexpr int cnt[7] = {4, 7, 11, 14, 18, 21, 25}, qb[8] = {0, 0, 4, 9, 14, 17, 17, 17};
      if (w == 0) waitvm<25 - cnt[s]>(); else waitvm<25 - cnt[s] + qb[s]>();
      bar();
      if (w != 0) {
        SFOR(m, qb[s], qb[s + 1])
          dma<0, false>(L_WQ + (w - 1 + 3 * m) * 1024, vq, sWQKV, zero);
          qpos += 3;
          const bool wrap = qpos >= 7;
          vq += wrap ? 28 * 2688 + 48 - 112 : 27 * 2688 + 48;
          qpos -= wrap ? 7 : 0;
        SEND
      }
      SFOR(kk, 0, 4)
        constexpr int ks = 4 * s + kk;
        const u32x4 xa = lds128(aX + ks * 32);
        const u32x4 bk = trfrag(aW + (16 * ks) * 224, aW + (16 * ks + 4) * 224);
        const u32x4 bv = trfrag(aW + (16 * ks) * 224 + 112, aW + (16 * ks + 4) * 224 + 112);
        ak = mfma32(xa, bk, ak);
        av = mfma32(xa, bv, av);
      SEND
    SEND
    STAMP(2);
    const u32 col = l & 31;
    u32x4 pk[2], pv[2];
    SFOR(i, 0, 8)
      const u32 a = cvtpk(phi(ak[2 * i]), phi(ak[2 * i + 1])), b = cvtpk(av[2 * i], av[2 * i + 1]);
      pk[i >> 2][i & 3] = col < 28 ? a : 0u;
      pv[i >> 2][i & 3] = col < 28 ? b : (col == 28 ? 0x3f803f80u : 0u);
    SEND
    f32x16 kv = {};
    kv = mfma32(pv[0], pk[0], kv);
    kv = mfma32(pv[1], pk[1], kv);
    const u32 a = L_KST + w * 4096 + hf * 512 + col * 4;
    SFOR(i, 0, 16) *(LAS float*)(a + ((i & 3) + 8 * (i >> 2)) * 128) = kv[i]; SEND
  }
  bar();
  if (w == 0) {
    const u32 quad = l & 7;
    SFOR(i, 0, 8)
      constexpr int hd = i >> 2;
      const u32 rho = 8 * (i & 3) + (l >> 3);
      const u32 e = rho < 16 ? rho : ((rho & 3) == 3 ? 28u : 16 + 3 * ((rho - 16) >> 2) + (rho & 3));
      const u32 a = L_KST + hd * 4096 + e * 128 + quad * 16;
      const f32x4 v = lds128f(a) + lds128f(a + 8192);
      st128(rWS, WS_KVP + ((j * 32 + (rho & ~1u) + hd) * 32 + p1) * 256 + (rho & 1) * 128 + quad * 16, __builtin_bit_cast(u32x4, v));
    SEND
    waitvm<0>();
    if (l == 0) set_flag(ws, F_KVP, id, tag);
  } else {
    waitvm<0>();
  }
  STAMP(3);
  bar();
  {
    f32x16 aq = {};
    const u32 aW = L_WQ + (8 * hf + trq) * 112 + 56 * hh + 32 * blk + 8 * trp;
    SFOR(ks, 0, 28)
      const u32x4 xa = lds128(aX + ks * 32);
      const u32x4 wq = trfrag(aW + (16 * ks) * 112, aW + (16 * ks + 4) * 112);
      aq = mfma32(wq, xa, aq);
    SEND
    u32x4 pq[2];
    SFOR(i, 0, 8)
      const u32 a = cvtpk(phi(aq[2 * i]), phi(aq[2 * i + 1]));
      pq[i >> 2][i & 3] = (i >= 6 && hf) ? 0u : a;
    SEND
    const u32 o = WS_PQ + ((p1 * 4 + 2 * th + blk) * 16 + 2 * j + hh) * 1024 + hf * 256 + (l & 15) * 16;
    st128(rWS, o, pq[0]);
    st128(rWS, o + 512, pq[1]);
  }
  waitvm<0>();
  bar();
  if (tid == 0) set_flag(ws, F_PQ, id, tag);
  STAMP(4);
  const u32x4 sWIN = make_srd(win_, 448 * 3584);
  const u32 vrow8 = l >> 3, vpos = l & 7;
  if (w != 0) {
    if (j < 7) {
      const u32x4 sWO = make_srd(wo_, 448 * 896);
      const u32 vo = vrow8 * 896 + (vpos ^ (((l >> 4) & 3) << 1)) * 16 + 128 * j;
      for (u32 n = w - 1; n < 56; n += 3) dma<0, false>(L_WO + n * 1024, vo, sWO, n * 8 * 896);
      const u32 vz = vrow8 * 3584 + (vpos ^ (((l >> 4) & 1) << 2)) * 16 + 128 * g14 + 1792;
      for (u32 n = w - 1; n < 56; n += 3) dma<0, false>(L_WZ2 + n * 1024, vz, sWIN, n * 8 * 3584);
    }
  } else {
    poll1(ws, flag_ptr(ws, F_KVP, (l & 31) * 8 + j), l < 32, tag);
    STAMP(5);
    u32x4 pr[8];
    const u32 vo = WS_KVP + (j * 32 + p1) * 8192 + l * 16;
    SFOR(i, 0, 4) ld128<i * 1024, true>(pr[i], vo, sWS, zero); SEND
    SFOR(i, 0, 4) ld128<i * 1024, true>(pr[4 + i], vo, sWS, 4096); SEND
    waitvm<0>();
    SFOR(i, 0, 8) pin(pr[i]); SEND
    f32x4 s = __builtin_bit_cast(f32x4, pr[0]);
    SFOR(i, 1, 8) s += __builtin_bit_cast(f32x4, pr[i]); SEND
    SFOR(e, 0, 4) s[e] = add_x32(add_x16(s[e])); SEND
    const u32 a = cvtpk(s[0], s[1]), b = cvtpk(s[2], s[3]);
    const u32 a2 = __builtin_amdgcn_update_dpp(0, (int)a, 0x4E, 0xf, 0xf, true), b2 = __builtin_amdgcn_update_dpp(0, (int)b, 0x4E, 0xf, 0xf, true);
    const u32 qd = l & 7, g = 2 * (qd >> 2) + (qd & 1), i = p1 >> 1, rq = (i & 7) >> 1;
    const u32 pos = 2 * (g ^ ((4 - rq) & 3)) + ((l >> 3) & 1);
    if (l < 16 && !(qd & 2)) st128(rWS, WS_KVR + ((2 * j + (p1 & 1)) * 16 + i) * 128 + pos * 16, u32x4{a, b, a2, b2});
    waitvm<0>();
    if (l == 0) set_flag(ws, F_KVR, id, tag);
    STAMP(6);
  }
  if (j == 7) {
    if (p1 < 28) {
      u32x4 wr[14];
      const u32 vo = (112 * w + vrow8) * 3584 + vpos * 16 + 128 * p1;
      SFOR(i, 0, 14) ld128<0, false>(wr[i], vo, sWIN, i * 8 * 3584); SEND
      waitvm<0>();
      f32x2 c[4] = {};
      SFOR(i, 0, 14) pin(wr[i]); SFOR(e, 0, 4) c[e] += f32x2{bflo(wr[i][e]), bfhi(wr[i][e])}; SEND SEND
      float cs[8];
      SFOR(e, 0, 8) cs[e] = add_x32(add_x16(dpp_add<0x128>(c[e >> 1][e & 1]))); SEND
      if (l < 8) {
        *(LAS f32x4*)(L_MISC + w * 256 + l * 32) = f32x4{cs[0], cs[1], cs[2], cs[3]};
        *(LAS f32x4*)(L_MISC + w * 256 + l * 32 + 16) = f32x4{cs[4], cs[5], cs[6], cs[7]};
      }
      bar();
      if (w == 0) {
        if (l < 16) {
          const u32 a = L_MISC + l * 16;
          const f32x4 t = (lds128f(a) + lds128f(a + 256)) + (lds128f(a + 512) + lds128f(a + 768));
          st128(rWS, WS_CS + p1 * 256 + l * 16, __builtin_bit_cast(u32x4, t));
        }
        waitvm<0>();
        if (l == 0) set_flag(ws, F_CS, p1, tag);
      }
    }
    TSOUT();
    return;
  }
  if (w == 0) {
    poll1(ws, flag_ptr(ws, F_PQ, p1 * 8 + (l & 7)), l < 8, tag);
    poll4(ws, flag_ptr(ws, F_KVR, l), tag);
    if (wg == 0 && l == 0) __hip_atomic_store((GAS u64*)(ws + WS_EPOCH), tag, RLX_AGENT);
  }
  bar();
  STAMP(7);
  const u32 t16 = l & 15, g4 = l >> 4;
  f32x4 hacc[4];
  {
    u32x4 kr[8], pq[16];
    u32x2 xt[4];
    const u32 vk = WS_KVR + w * 8192 + l * 16;
    SFOR(i, 0, 4) ld128<i * 1024, true>(kr[i], vk, sWS, zero); SEND
    SFOR(i, 0, 4) ld128<i * 1024, true>(kr[4 + i], vk, sWS, 4096); SEND
    const u32 vp = WS_PQ + (p1 * 4 + w) * 16384 + l * 16;
    SFOR(i, 0, 16) ld128<(i & 3) * 1024, true>(pq[i], vp, sWS, (i >> 2) * 4096); SEND
    const u32 vx = (64 * p1 + 16 * w + t16) * 896 + 128 * j + 8 * g4;
    SFOR(i, 0, 4) ld64<i * 32, false>(xt[i], vx, sX, zero); SEND
    waitvm<20>();
    SFOR(i, 0, 8) pin(kr[i]); *(LAS u32x4*)(L_KVR + w * 8192 + i * 1024 + l * 16) = kr[i]; SEND
    bar();
    STAMP(8);
    const u32 aK = L_KVR + (t16 >> 1) * 128 + (2 * (g4 ^ ((4 - (t16 >> 2)) & 3)) + (t16 & 1)) * 16;
    u32 aO[2][4];
    {
      const u32 r0 = 4 * g4 + trq, r1 = 16 + 3 * g4 + (trq < 2 ? trq : 2);
      SFOR(y, 0, 4)
        aO[0][y] = L_WO + r0 * 128 + ((y ^ ((r0 >> 1) & 3)) * 32) + 8 * trp;
        aO[1][y] = L_WO + r1 * 128 + ((y ^ ((r1 >> 1) & 3)) * 32) + 8 * trp;
      SEND
    }
    SFOR(n, 0, 4) hacc[n] = f32x4{0.f, 0.f, 0.f, 0.f}; SEND
    SFOR(h, 0, 16)
      waitpin<19 - h>(pq[h]);
      const f32x4 z4 = {0.f, 0.f, 0.f, 0.f};
      const f32x4 d0 = mfma16(lds128(aK + h * 2048), pq[h], z4), d1 = mfma16(lds128(aK + h * 2048 + 1024), pq[h], z4);
      const float r = __builtin_amdgcn_rcpf(d1[3] + 1e-6f);
      const u32x4 ob = {cvtpk(d0[0] * r, d0[1] * r), cvtpk(d0[2] * r, d0[3] * r), cvtpk(d1[0] * r, d1[1] * r), cvtpk(d1[2] * r, 0.f)};
      SFOR(n, 0, 4)
        constexpr int y = n ^ (2 * (h & 1));
        hacc[n] = mfma16(trfrag(aO[0][y] + h * 3584, aO[1][y] + h * 3584), ob, hacc[n]);
      SEND
    SEND
    waitvm<0>();
    float s = 0.f, q = 0.f;
    SFOR(n, 0, 4)
      pin(xt[n]);
      hacc[n] += f32x4{bflo(xt[n].x), bfhi(xt[n].x), bflo(xt[n].y), bfhi(xt[n].y)};
      SFOR(e, 0, 4) s += hacc[n][e]; q = __builtin_fmaf(hacc[n][e], hacc[n][e], q); SEND
    SEND
    const u32 o = WS_H + ((2 * p1 + (w >> 1)) * 28 + 4 * j + (g4 >> 1)) * 1024 + (g4 & 1) * 512 + (16 * (w & 1) + t16) * 16;
    SFOR(m, 0, 2)
      st128(rWS, o + m * 2048, u32x4{cvtpk(hacc[2 * m][0], hacc[2 * m][1]), cvtpk(hacc[2 * m][2], hacc[2 * m][3]), cvtpk(hacc[2 * m + 1][0], hacc[2 * m + 1][1]),
                                     cvtpk(hacc[2 * m + 1][2], hacc[2 * m + 1][3])});
    SEND
    s = add_x32(add_x16(s));
    q = add_x32(add_x16(q));
    if (l < 16) st64(rWS, WS_HST + ((P * 7 + j) * 128 + 64 * (p1 & 1) + 16 * w + l) * 8, u32x2{asu(s), asu(q)});
  }
  waitvm<0>();
  bar();
  if (tid == 0) set_flag(ws, F_H, id, tag);
  STAMP(9);
  if (w != 0) {
    const u32 vz = vrow8 * 3584 + (vpos ^ (((l >> 4) & 1) << 2)) * 16 + 128 * g14;
    for (u32 n = w - 1; n < 56; n += 3) dma<0, false>(L_WZ1 + n * 1024, vz, sWIN, n * 8 * 3584);
  } else {
    const u32 m = l < 14 ? l : 0, pp = 2 * P + (m >= 7), jj = m >= 7 ? m - 7 : m;
    GAS u64* fp = l < 14 ? flag_ptr(ws, F_H, pp * 8 + jj) : flag_ptr(ws, F_CS, g14 + (l & 1) * 14);
    poll1(ws, fp, l < 16, tag);
  }
  bar();
  STAMP(10);
  const u32 t32 = l & 31;
  const u32x4 sWSP = make_srd(wsp_, 2048 * 4096);
  u32 vS[2];
  SFOR(i, 0, 2) vS[i] = (128 * P + 32 * w + vrow8) * 4096 + (vpos ^ (4 * i + (l >> 4))) * 16; SEND
  const u32 spA = w * 8192, spB = w * 12288;
  auto sp_issue = [&](auto c_, auto s_) __attribute__((always_inline)) {
    constexpr int c = decltype(c_)::value, s = decltype(s_)::value;
    const u32 kb = ((P + (c >> 1)) & 15) * 256 + (c & 1) * 128;
    SFOR(i, 0, 4) dma<0, false>(sp_slot(s) + (s < 2 ? spA : spB) + i * 1024, vS[i & 1], sWSP, kb + i * 8 * 4096); SEND
  };
  u32 aS[2][4];
  SFOR(k, 0, 4)
    const u32 b = t32 * 128 + (((2 * k + hf) ^ ((t32 >> 1) & 7)) * 16);
    aS[0][k] = L_SPA + spA + b;
    aS[1][k] = L_SPB + spB + b;
  SEND
  u32x4 res[NRES * 8];
  auto sp_take = [&](auto c_, auto s_) __attribute__((always_inline)) {
    constexpr int c = decltype(c_)::value, s = decltype(s_)::value, o = (s < 2 ? s : s - 2) * 4096, r = (c - 2 * NSTREAM) * 4;
    take4<o, (r < 48)>(res[r], res[r + 1], res[r + 2], res[r + 3], aS[s >= 2][0], aS[s >= 2][1], aS[s >= 2][2], aS[s >= 2][3]);
  };
#define IC(v) std::integral_constant<int, (v)>{}
  u32 z1p[16];
  u32 bbits;
  {
    u32x4 hb[28];
    u32x2 st[4];
    u32x4 cs[8];
    const u32 vst = WS_HST + ((P * 7 + 4 * hf) * 128 + 32 * w + t32) * 8;
    SFOR(i, 0, 3) ld64<i * 1024, true>(st[i], vst, sWS, zero); SEND
    ld64<0, true>(st[3], vst + (hf ? 0 : 3072), sWS, zero);
    const u32 vh = WS_H + (4 * P + w) * 28 * 1024 + l * 16;
    SFOR(i, 0, 28) ld128<(i & 3) * 1024, true>(hb[i], vh, sWS, (i >> 2) * 4096); SEND
    const u32 vc2 = WS_CS + (896 + 64 * g14 + 4 * hf) * 4;
    SFOR(i, 0, 8) ld128<i * 32, true>(cs[i], vc2, sWS, zero); SEND
    {
      const u32x4 sB = make_srd(bsp_, 4096);
      asm volatile("buffer_load_ushort %0, %1, %2, 0 offen" : "=v"(bbits) : "v"((128 * P + 32 * w + t32) * 2), "s"(sB) : "memory");
    }
    const u32 rowp = (4 * hf + trq) * 128 + blk * 32;
    const u32 aZ2[2] = {L_WZ2 + rowp + ((trq >> 1) * 64) + 8 * trp, L_WZ2 + rowp + (((trq >> 1) ^ 1) * 64) + 8 * trp};
    const u32 pp = 8 * (2 * (trp & 1) + (trp >> 1));
    const u32 aZ1[2] = {L_WZ1 + rowp + ((trq >> 1) * 64) + pp, L_WZ1 + rowp + (((trq >> 1) ^ 1) * 64) + pp};
    f32x16 z[2] = {};
    SFOR(k, 0, 28)
      if constexpr ((k & 1) == 0) { waitpin<9 + 26 - k>(hb[k]); pin(hb[k + 1]); }
      constexpr int ro = (32 * (k >> 1) + 8 * (k & 1)) * 128;
      SFOR(f, 0, 2) z[f] = mfma32(trfrag(aZ2[f] + ro, aZ2[f] + ro + 2048), hb[k], z[f]); SEND
    SEND
    waitvm<0>();
    STAMP(11);
    SFOR(i, 0, 4) pin(st[i]); SEND
    SFOR(i, 0, 8) pin(cs[i]); SEND
    pin(bbits);
    float rs, nm;
    {
      float s = asf(st[0].x) + asf(st[1].x) + asf(st[2].x);
      float q = asf(st[0].y) + asf(st[1].y) + asf(st[2].y);
      s += hf ? 0.f : asf(st[3].x);
      q += hf ? 0.f : asf(st[3].y);
      s = add_x32(s) * (1.0f / 448);
      q = add_x32(q) * (1.0f / 448);
      rs = __builtin_amdgcn_rsqf(__builtin_fmaxf(q - s * s, 0.f) + 1e-5f);
      nm = -s * rs;
    }
    f32x2 zz[16];
    {
      f32x2 s2 = {0.f, 0.f}, q2 = {0.f, 0.f};
      SFOR(i, 0, 16)
        constexpr int f = i >> 3, e = (2 * i) & 15;
        const f32x4 c = __builtin_bit_cast(f32x4, cs[i >> 1]);
        const f32x2 u = __builtin_elementwise_fma(f32x2{c[(2 * i) & 3], c[((2 * i) & 3) + 1]}, f32x2{nm, nm}, f32x2{z[f][e], z[f][e + 1]} * f32x2{rs, rs});
        zz[i] = gelu2(u);
        s2 += zz[i];
        q2 = __builtin_elementwise_fma(zz[i], zz[i], q2);
      SEND
      const float s = add_x32(s2.x + s2.y), q = add_x32(q2.x + q2.y);
      if (l < 32) st64(rWS, WS_ZST + ((P * 14 + g14) * 128 + 32 * w + l) * 8, u32x2{asu(s), asu(q)});
    }
    waitvm<0>();
    bar();
    if (tid == 0) set_flag(ws, F_ZS, id, tag);
    STAMP(12);
    const u32 vc1 = WS_CS + (64 * g14 + 8 * hf) * 4;
    SFOR(i, 0, 8) ld128<(i >> 2) * 128 + ((i >> 1) & 1) * 64 + (i & 1) * 16, true>(cs[i], vc1, sWS, zero); SEND
    SFOR(s, 0, 5) sp_issue(IC(2 * NSTREAM + s), IC(s)); SEND
    z[0] = f32x16{};
    z[1] = f32x16{};
    SFOR(k, 0, 28)
      constexpr int ro = (32 * (k >> 1) + 8 * (k & 1)) * 128;
      SFOR(f, 0, 2) z[f] = mfma32(trfrag(aZ1[f] + ro, aZ1[f] + ro + 2048), hb[k], z[f]); SEND
      if constexpr (k >= 8 && (k & 3) == 0) {
        constexpr int s = (k - 8) >> 2;
        waitvm<16>();
        sp_take(IC(2 * NSTREAM + s), IC(s));
        sp_issue(IC(2 * NSTREAM + 5 + s), IC(s));
      }
    SEND
    STAMP(13);
    if (w == 0) {
      const u32 m = l < 14 ? l : 0, pp2 = 2 * P + (m >= 7), jj = m >= 7 ? m - 7 : m;
      poll1(ws, flag_ptr(ws, F_ZS, pp2 * 8 + jj), l < 14, tag);
    }
    bar();
    STAMP(14);
    {
      u32x2 zs[7];
      const u32 vz = WS_ZST + ((P * 14 + 7 * hf) * 128 + 32 * w + t32) * 8;
      SFOR(i, 0, 7) ld64<(i & 3) * 1024, true>(zs[i], vz, sWS, (i >> 2) * 4096); SEND
      waitvm<0>();
      float s = 0.f, q = 0.f;
      SFOR(i, 0, 7) pin(zs[i]); s += asf(zs[i].x); q += asf(zs[i].y); SEND
      s = add_x32(s) * (1.0f / 896);
      q = add_x32(q) * (1.0f / 896);
      const float r2 = __builtin_amdgcn_rsqf(__builtin_fmaxf(q - s * s, 0.f) + 1e-5f), n2 = -s * r2;
      const u32 o = WS_Z + (g14 * 64 + 4 * P + w) * 4096 + hf * 512;
      SFOR(c, 0, 4)
        u32x4 v;
        SFOR(e, 0, 4) const f32x2 t = __builtin_elementwise_fma(zz[4 * c + e], f32x2{r2, r2}, f32x2{n2, n2}); v[e] = cvtpk(t.x, t.y); SEND
        st128(rWS, o + c * 1024 + (t32 ^ ((2 * (c & 1) + hf) << 2)) * 16, v);
      SEND
    }
    waitvm<0>();
    bar();
    if (tid == 0) set_flag(ws, F_Z, id, tag);
    STAMP(15);
    SFOR(s, 0, 5) sp_take(IC(2 * NSTREAM + 5 + s), IC(s)); if constexpr (10 + s < 2 * NRES) sp_issue(IC(2 * NSTREAM + 10 + s), IC(s)); SEND
    SFOR(i, 0, 8) pin(cs[i]); SEND
    SFOR(i, 0, 16)
      constexpr int f = i >> 3, e = (2 * i) & 15;
      const f32x4 c = __builtin_bit_cast(f32x4, cs[i >> 1]);
      const f32x2 u = __builtin_elementwise_fma(f32x2{c[(2 * i) & 3], c[((2 * i) & 3) + 1]}, f32x2{nm, nm}, f32x2{z[f][e], z[f][e + 1]} * f32x2{rs, rs});
      const f32x2 t = gelu2(u);
      z1p[i] = cvtpk(t.x, t.y);
    SEND
  }
  static_assert(NRES == 10, "the schedule below is written for 20 resident chunks");
  SFOR(s, 0, 5) waitvm<16>(); sp_take(IC(2 * NSTREAM + 10 + s), IC(s)); sp_issue(IC(2 * NSTREAM + 15 + s), IC(s)); SEND
  SFOR(s, 0, 5) waitvm<16>(); sp_take(IC(2 * NSTREAM + 15 + s), IC(s)); sp_issue(IC(s), IC(s)); SEND
  STAMP(16);
  if (w == 0) poll1(ws, flag_ptr(ws, F_Z, (2 * (l & 15) + (p1 & 1)) * 8 + j), l < 16, tag);
  bar();
  STAMP(17);
  {
    const u32 vZ = WS_Z + g14 * 64 * 4096 + w * 4096 + l * 16;
    auto z_issue = [&](auto st_) __attribute__((always_inline)) {
      constexpr int st = decltype(st_)::value;
      const u32 so = ((P + st) & 15) * 16384, la = L_ZR + (st & 3) * 16384 + w * 4096;
      SFOR(i, 0, 4) dma<i * 1024, true>(la, vZ, sWS, so); SEND
    };
    SFOR(st, 0, 3) z_issue(IC(st)); SEND
    u32 aZ[4];
    {
      const u32 o3 = 2 * blk + (trp >> 1), b0 = L_ZR + o3 * 512 + (((2 * hf) ^ o3) * 4 + trq) * 16 + (trp & 1) * 8;
      SFOR(v, 0, 4) aZ[v] = b0 ^ (64 * (v & 1)) ^ (256 * (v >> 1)); SEND
    }
    f32x16 acc[2] = {};
    SFOR(st, 0, 16)
      constexpr int spc = (st >= 1 && 2 * (st - 1) + 5 < 2 * NSTREAM ? 1 : 0) + (st >= 1 && 2 * (st - 1) + 6 < 2 * NSTREAM ? 1 : 0)
                        + (st >= 2 && 2 * (st - 2) + 5 < 2 * NSTREAM ? 1 : 0) + (st >= 2 && 2 * (st - 2) + 6 < 2 * NSTREAM ? 1 : 0)
                        + (st >= 3 && 2 * (st - 3) + 5 < 2 * NSTREAM ? 1 : 0) + (st >= 3 && 2 * (st - 3) + 6 < 2 * NSTREAM ? 1 : 0);
      constexpr int zl = (st + 1 < 16 ? 1 : 0) + (st + 2 < 16 ? 1 : 0);
      waitvm<4 * (zl + spc)>();
      bar();
      if constexpr (st + 3 < 16) z_issue(IC(st + 3));
      SFOR(cc, 0, 2)
        constexpr int c = 2 * st + cc;
        u32x4 bs[4];
        if constexpr (st < NSTREAM) {
          constexpr int s = c % 5;
          if constexpr (c >= 5) {
            constexpr int ist = (c - 5) >> 1;
            constexpr int zy = (st - ist) - ((st + 3 < 16) ? 0 : 0);
            constexpr int cy = (c + 4 < 2 * NSTREAM ? 4 : 2 * NSTREAM - 1 - c);
            waitvm<4 * (zy + cy)>();
          }
          SFOR(k, 0, 4) bs[k] = lds128(aS[s >= 2][k] + (s < 2 ? s : s - 2) * 4096); SEND
          if constexpr (c + 5 < 2 * NSTREAM) {
            asm volatile("s_waitcnt lgkmcnt(0)" : "+v"(bs[0]), "+v"(bs[1]), "+v"(bs[2]), "+v"(bs[3])::"memory");
            sp_issue(IC(c + 5), IC(s));
          }
        }
        SFOR(k, 0, 4)
          constexpr int ks = 4 * cc + k, off = (st & 3) * 16384 + (ks >> 1) * 4096;
          SFOR(f, 0, 2)
            const u32x4 a = trfrag(aZ[2 * (ks & 1)] + off + f * 2048, aZ[2 * (ks & 1) + 1] + off + f * 2048);
            if constexpr (st < NSTREAM) acc[f] = mfma32(a, bs[k], acc[f]);
            else acc[f] = mfma32(a, res[(c - 2 * NSTREAM) * 4 + k], acc[f]);
          SEND
        SEND
      SEND
    SEND
    STAMP(18);
    const float b = bflo(bbits);
    const u32 o = WS_Y + ((8 * P + 2 * w + blk) * 28 + 2 * g14) * 1024 + hf * 256 + (l & 15) * 16;
    SFOR(c, 0, 4)
      u32x4 v;
      SFOR(e, 0, 4)
        constexpr int i = 8 * (c & 1) + 2 * e;
        const u32 zp = z1p[4 * c + e];
        v[e] = cvtpk(bflo(zp) * (acc[c >> 1][i] + b), bfhi(zp) * (acc[c >> 1][i + 1] + b));
      SEND
      st128(rWS, o + (c >> 1) * 1024 + (c & 1) * 512, v);
    SEND
  }
  waitvm<0>();
  bar();
  if (tid == 0) set_flag(ws, F_Y, id, tag);
  STAMP(19);
  if (w != 0) {
    const u32x4 sWOUT = make_srd(wout_, 896 * 896);
    const u32 x0 = (l >> 4) & 1;
    const u32 vo0 = vrow8 * 896 + (vpos ^ (x0 << 1)) * 16 + 128 * j, vo1 = vrow8 * 896 + (vpos ^ ((x0 | 2) << 1)) * 16 + 128 * j;
    for (u32 n = w - 1; n < 112; n += 3) dma<0, false>(L_WOUT + n * 1024, (n & 1) ? vo1 : vo0, sWOUT, n * 8 * 896);
  } else {
    const u32 m = l < 14 ? l : 0, pp = 2 * P + (m >= 7), jj = m >= 7 ? m - 7 : m;
    poll1(ws, flag_ptr(ws, F_Y, pp * 8 + jj), l < 14, tag);
  }
  bar();
  STAMP(20);
  {
    u32x4 yb[28];
    const u32 vy = WS_Y + (4 * p1 + w) * 28 * 1024 + l * 16;
    SFOR(i, 0, 28) ld128<(i & 3) * 1024, true>(yb[i], vy, sWS, (i >> 2) * 4096); SEND
    waitvm<28>();
    bar();
    STAMP(21);
    u32 aW[4];
    {
      const u32 xs = (trq >> 1) | ((g4 & 1) << 1);
      SFOR(n, 0, 4) aW[n] = L_WOUT + (8 * g4 + trq) * 128 + ((n ^ xs) * 32) + 8 * trp; SEND
    }
    SFOR(k, 0, 28)
      if constexpr ((k & 1) == 0) { waitpin<26 - k>(yb[k]); pin(yb[k + 1]); }
      SFOR(n, 0, 4) hacc[n] = mfma16(trfrag(aW[n] + k * 4096, aW[n] + k * 4096 + 512), yb[k], hacc[n]); SEND
    SEND
    STAMP(22);
    const __amdgpu_buffer_rsrc_t rO = __builtin_amdgcn_make_buffer_rsrc(out_, 0, 2048 * 896, 0x00020000);
    const u32 o = (64 * p1 + 16 * w + t16) * 896 + 128 * j + (g4 & 1) * 32 + (g4 >> 1) * 16;
    SFOR(m, 0, 2)
      const auto r0 = __builtin_amdgcn_permlane16_swap(cvtpk(hacc[2 * m][0], hacc[2 * m][1]), cvtpk(hacc[2 * m + 1][0], hacc[2 * m + 1][1]), false, false);
      const auto r1 = __builtin_amdgcn_permlane16_swap(cvtpk(hacc[2 * m][2], hacc[2 * m][3]), cvtpk(hacc[2 * m + 1][2], hacc[2 * m + 1][3]), false, false);
      __builtin_amdgcn_raw_buffer_store_b128(u32x4{(u32)r0[0], (u32)r1[0], (u32)r0[1], (u32)r1[1]}, rO, o + m * 64, 0, 0);
    SEND
  }
  STAMP(23);
  TSOUT();
  if (wg == 0) { const u32 until = ts[23] + 10 * (ts[AMP] - ts[0]); while ((int)((u32)__builtin_amdgcn_s_memrealtime() - until) < 0) __builtin_amdgcn_s_sleep(2); }
}

}

extern "C" void kernel_launch(void* const* d_in, const int* in_sizes, int n_in, void* d_out, int out_size, void* d_ws, size_t ws_size, hipStream_t stream) {
  fused_kernel<<<256, 256, 0, stream>>>(d_in[0], d_in[1], d_in[2], d_in[4], d_in[5], d_in[6], d_in[7], d_out, (char*)d_ws);
}
```

```cpp
#define TS
#include <hip/hip_runtime.h>
#include <hip/hip_bf16.h>
#include <stdint.h>
#include <type_traits>
#pragma clang diagnostic ignored "-Wint-to-pointer-cast"

namespace {

#define DI __device__ __forceinline__
#define LAS __attribute__((address_space(3)))
#define GAS __attribute__((address_space(1)))
typedef unsigned u32;
typedef unsigned long long u64;
typedef u32 u32x2 __attribute__((ext_vector_type(2)));
typedef u32 u32x4 __attribute__((ext_vector_type(4)));
typedef float f32x2 __attribute__((ext_vector_type(2)));
typedef float f32x4 __attribute__((ext_vector_type(4)));
typedef float f32x16 __attribute__((ext_vector_type(16)));
typedef __bf16 bf16x8 __attribute__((ext_vector_type(8)));
typedef __bf16 bf16x2 __attribute__((ext_vector_type(2)));
typedef short s16x4 __attribute__((ext_vector_type(4)));
#define RLX_AGENT __ATOMIC_RELAXED, __HIP_MEMORY_SCOPE_AGENT

constexpr u32 WS_EPOCH = 0, WS_TMO = 128;
constexpr u32 WS_FLAGS = 4096;
constexpr u32 F_KVP = 0, F_KVR = 1, F_PQ = 2, F_H = 3, F_ZS = 4, F_Z = 5, F_Y = 6, F_CS = 7;
constexpr u32 WS_KVP = 1u << 20;
constexpr u32 WS_KVR = WS_KVP + (2u << 20);
constexpr u32 WS_CS = WS_KVR + 32768;
constexpr u32 WS_HST = WS_CS + 8192;
constexpr u32 WS_ZST = WS_HST + 16 * 7 * 128 * 8;
constexpr u32 WS_PQ = 4u << 20;
constexpr u32 WS_H = 6u << 20;
constexpr u32 WS_Z = 8u << 20;
constexpr u32 WS_Y = 12u << 20;
constexpr u32 WS_DBG = 16u << 20;
constexpr u32 WS_BYTES = 17u << 20;
static_assert(WS_ZST + 16 * 14 * 128 * 8 <= WS_PQ, "ws map");

constexpr u32 L_X = 0, XSTR = 912;
constexpr u32 L_WKV = 64 * XSTR;
constexpr u32 L_WQ = L_WKV;
constexpr u32 L_KST = L_WKV + 53248;
constexpr u32 L_WO = 0;
constexpr u32 L_WZ2 = 57344;
constexpr u32 L_WZ1 = 0;
constexpr u32 L_KVR = 114688;
constexpr u32 L_ZR = 0;
constexpr u32 L_SPA = 114688, L_SPB = 65536;
constexpr u32 L_WOUT = 0;
constexpr u32 L_MISC = 160768;
constexpr u32 LDS_BYTES = 163840;

constexpr u32 SPIN_CAP = 1u << 16;
constexpr float LOG2E = 1.4426950408889634f;

DI u32 rfl(u32 x) { return __builtin_amdgcn_readfirstlane(x); }
DI u32x4 make_srd(const void* p, u32 bytes) {
  const u64 a = (u64)p;
  return u32x4{(u32)a, (u32)(a >> 32) & 0xffffu, bytes, 0x00020000u};
}
DI u32 cvtpk(float lo, float hi) {
  f32x2 v = {lo, hi};
  return __builtin_bit_cast(u32, __builtin_convertvector(v, bf16x2));
}
DI float asf(u32 x) { return __builtin_bit_cast(float, x); }
DI u32 asu(float x) { return __builtin_bit_cast(u32, x); }
DI float bflo(u32 x) { return __builtin_bit_cast(float, x << 16); }
DI float bfhi(u32 x) { return __builtin_bit_cast(float, x & 0xffff0000u); }
DI f32x16 mfma32(u32x4 a, u32x4 b, f32x16 c) {
  return __builtin_amdgcn_mfma_f32_32x32x16_bf16(__builtin_bit_cast(bf16x8, a), __builtin_bit_cast(bf16x8, b), c, 0, 0, 0);
}
DI f32x4 mfma16(u32x4 a, u32x4 b, f32x4 c) {
  return __builtin_amdgcn_mfma_f32_16x16x32_bf16(__builtin_bit_cast(bf16x8, a), __builtin_bit_cast(bf16x8, b), c, 0, 0, 0);
}
DI u32x2 tr64(u32 a) { return __builtin_bit_cast(u32x2, __builtin_amdgcn_ds_read_tr16_b64_v4i16((LAS s16x4*)a)); }
DI u32x4 trfrag(u32 a0, u32 a1) {
  const u32x2 lo = tr64(a0), hi = tr64(a1);
  return u32x4{lo.x, lo.y, hi.x, hi.y};
}
DI u32x4 lds128(u32 a) { return *(LAS u32x4*)a; }
DI f32x4 lds128f(u32 a) { return *(LAS f32x4*)a; }
DI void bar() { asm volatile("s_waitcnt lgkmcnt(0)\n\ts_barrier" ::: "memory"); }
template <int N> DI void waitvm() { asm volatile("s_waitcnt vmcnt(%0)" ::"i"(N) : "memory"); }
template <int IMM, bool SC1> DI void dma(u32 ldsaddr, u32 voff, u32x4 srd, u32 soff) {
  if constexpr (SC1)
    asm volatile("s_mov_b32 m0, %0\n\ts_nop 0\n\tbuffer_load_dwordx4 %1, %2, %3 offen offset:%4 sc1 lds" ::"s"(ldsaddr), "v"(voff), "s"(srd), "s"(soff), "i"(IMM) : "memory");
  else
    asm volatile("s_mov_b32 m0, %0\n\ts_nop 0\n\tbuffer_load_dwordx4 %1, %2, %3 offen offset:%4 lds" ::"s"(ldsaddr), "v"(voff), "s"(srd), "s"(soff), "i"(IMM) : "memory");
}
template <int IMM, bool SC1> DI void ld128(u32x4& d, u32 voff, u32x4 srd, u32 soff) {
  if constexpr (SC1) asm volatile("buffer_load_dwordx4 %0, %1, %2, %3 offen offset:%4 sc1" : "=v"(d) : "v"(voff), "s"(srd), "s"(soff), "i"(IMM) : "memory");
  else asm volatile("buffer_load_dwordx4 %0, %1, %2, %3 offen offset:%4" : "=v"(d) : "v"(voff), "s"(srd), "s"(soff), "i"(IMM) : "memory");
}
template <int IMM, bool SC1> DI void ld64(u32x2& d, u32 voff, u32x4 srd, u32 soff) {
  if constexpr (SC1) asm volatile("buffer_load_dwordx2 %0, %1, %2, %3 offen offset:%4 sc1" : "=v"(d) : "v"(voff), "s"(srd), "s"(soff), "i"(IMM) : "memory");
  else asm volatile("buffer_load_dwordx2 %0, %1, %2, %3 offen offset:%4" : "=v"(d) : "v"(voff), "s"(srd), "s"(soff), "i"(IMM) : "memory");
}
template <int N, typename T> DI void waitpin(T& a) { asm volatile("s_waitcnt vmcnt(%1)" : "+v"(a) : "i"(N) : "memory"); }
template <typename T> DI void pin(T& a) { asm volatile("" : "+v"(a)); }
template <int OFF, bool ACC> DI void take4(u32x4& r0, u32x4& r1, u32x4& r2, u32x4& r3, u32 a0, u32 a1, u32 a2, u32 a3) {
  if constexpr (ACC)
    asm volatile("ds_read_b128 %0, %4 offset:%8\n\tds_read_b128 %1, %5 offset:%8\n\tds_read_b128 %2, %6 offset:%8\n\tds_read_b128 %3, %7 offset:%8\n\ts_waitcnt lgkmcnt(0)"
                 : "=&a"(r0), "=&a"(r1), "=&a"(r2), "=&a"(r3) : "v"(a0), "v"(a1), "v"(a2), "v"(a3), "i"(OFF) : "memory");
  else
    asm volatile("ds_read_b128 %0, %4 offset:%8\n\tds_read_b128 %1, %5 offset:%8\n\tds_read_b128 %2, %6 offset:%8\n\tds_read_b128 %3, %7 offset:%8\n\ts_waitcnt lgkmcnt(0)"
                 : "=&v"(r0), "=&v"(r1), "=&v"(r2), "=&v"(r3) : "v"(a0), "v"(a1), "v"(a2), "v"(a3), "i"(OFF) : "memory");
}
DI void st128(__amdgpu_buffer_rsrc_t rs, u32 off, u32x4 v) { __builtin_amdgcn_raw_buffer_store_b128(v, rs, off, 0, 16); }
DI void st64(__amdgpu_buffer_rsrc_t rs, u32 off, u32x2 v) { __builtin_amdgcn_raw_buffer_store_b64(v, rs, off, 0, 16); }
template <int CTRL> DI float dpp_add(float x) {
  return x + __builtin_bit_cast(float, __builtin_amdgcn_update_dpp(0, __builtin_bit_cast(int, x), CTRL, 0xf, 0xf, true));
}
DI float add_x16(float x) {
  const auto r = __builtin_amdgcn_permlane16_swap(__builtin_bit_cast(u32, x), __builtin_bit_cast(u32, x), false, false);
  return __builtin_bit_cast(float, (u32)r[0]) + __builtin_bit_cast(float, (u32)r[1]);
}
DI float add_x32(float x) {
  const auto r = __builtin_amdgcn_permlane32_swap(__builtin_bit_cast(u32, x), __builtin_bit_cast(u32, x), false, false);
  return __builtin_bit_cast(float, (u32)r[0]) + __builtin_bit_cast(float, (u32)r[1]);
}
DI float phi(float t) { return (t > 0.f ? t + 1.0f : __builtin_amdgcn_exp2f(t * LOG2E)) + 1e-6f; }
DI f32x2 gelu2(f32x2 x) {
  const float c1 = -2.0f * 0.7978845608028654f * LOG2E, c3 = c1 * 0.044715f;
  const f32x2 p = __builtin_elementwise_fma(x * x, f32x2{c3, c3}, f32x2{c1, c1}) * x;
  const f32x2 e = f32x2{__builtin_amdgcn_exp2f(p.x), __builtin_amdgcn_exp2f(p.y)} + f32x2{1.f, 1.f};
  return x * f32x2{__builtin_amdgcn_rcpf(e.x), __builtin_amdgcn_rcpf(e.y)};
}

DI GAS u64* flag_ptr(char* ws, u32 arr, u32 idx) { return (GAS u64*)(ws + WS_FLAGS + (arr * 256 + idx) * 128); }
DI void set_flag(char* ws, u32 arr, u32 idx, u64 tag) { __hip_atomic_store(flag_ptr(ws, arr, idx), tag, RLX_AGENT); }
DI void poll1(char* ws, GAS u64* p, bool on, u64 tag) {
  for (u32 spins = 0;; ++spins) {
    u64 v = tag;
    if (on) v = __hip_atomic_load(p, RLX_AGENT);
    if (__all(v == tag)) break;
    if ((spins & 63u) == 63u) {
      if (spins > SPIN_CAP) __hip_atomic_store((GAS u64*)(ws + WS_TMO), tag, RLX_AGENT);
      if (__hip_atomic_load((GAS u64*)(ws + WS_TMO), RLX_AGENT) == tag) break;
    }
  }
  __builtin_amdgcn_fence(__ATOMIC_ACQUIRE, "wavefront");
}
DI void poll4(char* ws, GAS u64* p, u64 tag) {
  for (u32 spins = 0;; ++spins) {
    const u64 a = __hip_atomic_load(p, RLX_AGENT), b = __hip_atomic_load(p + 64 * 16, RLX_AGENT), c = __hip_atomic_load(p + 128 * 16, RLX_AGENT),
              d = __hip_atomic_load(p + 192 * 16, RLX_AGENT);
    if (__all(a == tag && b == tag && c == tag && d == tag)) break;
    if ((spins & 63u) == 63u) {
      if (spins > SPIN_CAP) __hip_atomic_store((GAS u64*)(ws + WS_TMO), tag, RLX_AGENT);
      if (__hip_atomic_load((GAS u64*)(ws + WS_TMO), RLX_AGENT) == tag) break;
    }
  }
  __builtin_amdgcn_fence(__ATOMIC_ACQUIRE, "wavefront");
}

#define STAMP(i) do { __builtin_amdgcn_sched_barrier(0); ts[i] = (u32)__builtin_amdgcn_s_memrealtime(); asm volatile("s_waitcnt lgkmcnt(0)" : "+s"(ts[i])); __builtin_amdgcn_sched_barrier(0); } while (0)
#define TSOUT() do { if (tid == 0) { _Pragma("unroll") for (int i_ = 0; i_ < 24; ++i_) ((u32*)(ws + WS_DBG))[wg * 32 + i_] = ts[i_]; } } while (0)

template <int I, int N, typename F> DI void sfor(F&& f) {
  if constexpr (I < N) {
    f(std::integral_constant<int, I>{});
    sfor<I + 1, N>(f);
  }
}
#define SFOR(i, a, b) sfor<a, b>([&](auto i##_c) __attribute__((always_inline)) { constexpr int i = decltype(i##_c)::value;
#define SEND });

constexpr int NRES = 10, NSTREAM = 16 - NRES;
constexpr u32 sp_slot(int s) { return s < 2 ? L_SPA + s * 4096 : L_SPB + (s - 2) * 4096; }

__global__ void __launch_bounds__(256, 1)
fused_kernel(const void* __restrict__ x_, const void* __restrict__ wqkv_, const void* __restrict__ wo_, const void* __restrict__ win_,
             const void* __restrict__ wsp_, const void* __restrict__ bsp_, const void* __restrict__ wout_, void* __restrict__ out_, char* __restrict__ ws) {
  __shared__ __attribute__((aligned(1024))) char smem[LDS_BYTES];
  asm volatile("" ::"v"((u32)(uintptr_t)(LAS char*)smem));
  const u32 tid = threadIdx.x, l = tid & 63, w = rfl(tid >> 6), wg = blockIdx.x;
  const u32 xcd = wg & 7, rr_ = wg >> 3;
  const u32 p1 = 8 * (xcd >> 1) + (rr_ & 7), j = 4 * (xcd & 1) + (rr_ >> 3);
  const u32 id = p1 * 8 + j;
  const u32 P = p1 >> 1, g14 = 2 * j + (p1 & 1);
  u32 ts[24] = {};
  STAMP(0);
  const u32x4 sX = make_srd(x_, 2048 * 896), sWQKV = make_srd(wqkv_, 448 * 2688), sWS = make_srd(ws, WS_BYTES);
  const __amdgpu_buffer_rsrc_t rWS = __builtin_amdgcn_make_buffer_rsrc(ws, 0, WS_BYTES, 0x00020000);
  const u32 zero = 0;

  u32x2 epoch;
  ld64<0, true>(epoch, zero, sWS, WS_EPOCH);
  u32x4 xr[2][7];
  {
    const u32 vo = (64 * p1 + 16 * w + (l >> 3)) * 896 + (l & 7) * 16;
    SFOR(i, 0, 7) ld128<i * 128, false>(xr[0][i], vo, sX, zero); SEND
    SFOR(i, 0, 7) ld128<i * 128, false>(xr[1][i], vo, sX, 8 * 896); SEND
  }
  {
    u32 vkv[7];
    SFOR(m, 0, 7)
      const u32 c = 64 * w + l + 256 * m, row = c / 14, pos = c - 14 * row;
      vkv[m] = row * 2688 + 896 + 16 * pos + (pos >= 7 ? 784 : 0) + 112 * j;
    SEND
    SFOR(m, 0, 25) dma<0, false>(L_WKV + (w + 4 * m) * 1024, vkv[m % 7], sWQKV, (m / 7) * 128 * 2688); SEND
  }
  waitpin<39>(epoch);
  const u64 tag = (((u64)rfl(epoch.y) << 32) | rfl(epoch.x)) + 1;
  SFOR(r, 0, 2)
    waitvm<25 + (1 - r) * 7>();
    SFOR(i, 0, 7) pin(xr[r][i]); SEND
    f32x2 v[28];
    SFOR(i, 0, 7) SFOR(e, 0, 4) v[4 * i + e] = f32x2{bflo(xr[r][i][e]), bfhi(xr[r][i][e])}; SEND SEND
    f32x2 s2 = v[0];
    SFOR(i, 1, 28) s2 += v[i]; SEND
    float s = s2.x + s2.y;
    s = dpp_add<0xB1>(s); s = dpp_add<0x4E>(s); s = dpp_add<0x141>(s);
    const float mean = s * (1.0f / 448);
    f32x2 q2 = {0.f, 0.f};
    SFOR(i, 0, 28) v[i] -= f32x2{mean, mean}; q2 = __builtin_elementwise_fma(v[i], v[i], q2); SEND
    float q = q2.x + q2.y;
    q = dpp_add<0xB1>(q); q = dpp_add<0x4E>(q); q = dpp_add<0x141>(q);
    const float rs = __builtin_amdgcn_rsqf(q * (1.0f / 448) + 1e-5f);
    const u32 a = L_X + (16 * w + 8 * r + (l >> 3)) * XSTR + (l & 7) * 16;
    SFOR(i, 0, 7)
      u32x4 o;
      SFOR(e, 0, 4) const f32x2 t = v[4 * i + e] * f32x2{rs, rs}; o[e] = cvtpk(t.x, t.y); SEND
      *(LAS u32x4*)(a + i * 128) = o;
    SEND
  SEND
  STAMP(1);
  const u32 hh = w & 1, th = w >> 1, hf = l >> 5;
  const u32 aX = L_X + (32 * th + (l & 31)) * XSTR + hf * 16;
  const u32 trq = (l >> 2) & 3, trp = l & 3, blk = (l >> 4) & 1;
  {
    f32x16 ak = {}, av = {};
    const u32 aW = L_WKV + (8 * hf + trq) * 224 + 56 * hh + 32 * blk + 8 * trp;
    u32 qc = 64 * (w - 1) + l, qrow = qc / 7, qpos = qc - 7 * qrow;
    u32 vq = qrow * 2688 + 16 * qpos + 112 * j;
    SFOR(s, 0, 7)
      constexpr int cnt[7] = {4, 7, 11, 14, 18, 21, 25}, qb[8] = {0, 0, 4, 9, 14, 17, 17, 17};
      if (w == 0) waitvm<25 - cnt[s]>(); else waitvm<25 - cnt[s] + qb[s]>();
      bar();
      if (w != 0) {
        SFOR(m, qb[s], qb[s + 1])
          dma<0, false>(L_WQ + (w - 1 + 3 * m) * 1024, vq, sWQKV, zero);
          qpos += 3;
          const bool wrap = qpos >= 7;
          vq += wrap ? 28 * 2688 + 48 - 112 : 27 * 2688 + 48;
          qpos -= wrap ? 7 : 0;
        SEND
      }
      SFOR(kk, 0, 4)
        constexpr int ks = 4 * s + kk;
        const u32x4 xa = lds128(aX + ks * 32);
        const u32x4 bk = trfrag(aW + (16 * ks) * 224, aW + (16 * ks + 4) * 224);
        const u32x4 bv = trfrag(aW + (16 * ks) * 224 + 112, aW + (16 * ks + 4) * 224 + 112);
        ak = mfma32(xa, bk, ak);
        av = mfma32(xa, bv, av);
      SEND
    SEND
    STAMP(2);
    const u32 col = l & 31;
    u32x4 pk[2], pv[2];
    SFOR(i, 0, 8)
      const u32 a = cvtpk(phi(ak[2 * i]), phi(ak[2 * i + 1])), b = cvtpk(av[2 * i], av[2 * i + 1]);
      pk[i >> 2][i & 3] = col < 28 ? a : 0u;
      pv[i >> 2][i & 3] = col < 28 ? b : (col == 28 ? 0x3f803f80u : 0u);
    SEND
    f32x16 kv = {};
    kv = mfma32(pv[0], pk[0], kv);
    kv = mfma32(pv[1], pk[1], kv);
    const u32 a = L_KST + w * 4096 + hf * 512 + col * 4;
    SFOR(i, 0, 16) *(LAS float*)(a + ((i & 3) + 8 * (i >> 2)) * 128) = kv[i]; SEND
  }
  bar();
  if (w == 0) {
    const u32 quad = l & 7;
    SFOR(i, 0, 8)
      constexpr int hd = i >> 2;
      const u32 rho = 8 * (i & 3) + (l >> 3);
      const u32 e = rho < 16 ? rho : ((rho & 3) == 3 ? 28u : 16 + 3 * ((rho - 16) >> 2) + (rho & 3));
      const u32 a = L_KST + hd * 4096 + e * 128 + quad * 16;
      const f32x4 v = lds128f(a) + lds128f(a + 8192);
      st128(rWS, WS_KVP + ((j * 32 + (rho & ~1u) + hd) * 32 + p1) * 256 + (rho & 1) * 128 + quad * 16, __builtin_bit_cast(u32x4, v));
    SEND
    waitvm<0>();
    if (l == 0) set_flag(ws, F_KVP, id, tag);
  } else {
    waitvm<0>();
  }
  STAMP(3);
  bar();
  {
    f32x16 aq = {};
    const u32 aW = L_WQ + (8 * hf + trq) * 112 + 56 * hh + 32 * blk + 8 * trp;
    SFOR(ks, 0, 28)
      const u32x4 xa = lds128(aX + ks * 32);
      const u32x4 wq = trfrag(aW + (16 * ks) * 112, aW + (16 * ks + 4) * 112);
      aq = mfma32(wq, xa, aq);
    SEND
    u32x4 pq[2];
    SFOR(i, 0, 8)
      const u32 a = cvtpk(phi(aq[2 * i]), phi(aq[2 * i + 1]));
      pq[i >> 2][i & 3] = (i >= 6 && hf) ? 0u : a;
    SEND
    const u32 o = WS_PQ + ((p1 * 4 + 2 * th + blk) * 16 + 2 * j + hh) * 1024 + hf * 256 + (l & 15) * 16;
    st128(rWS, o, pq[0]);
    st128(rWS, o + 512, pq[1]);
  }
  waitvm<0>();
  bar();
  if (tid == 0) set_flag(ws, F_PQ, id, tag);
  STAMP(4);
  const u32x4 sWIN = make_srd(win_, 448 * 3584);
  const u32 vrow8 = l >> 3, vpos = l & 7;
  if (w != 0) {
    if (j < 7) {
      const u32x4 sWO = make_srd(wo_, 448 * 896);
      const u32 vo = vrow8 * 896 + (vpos ^ (((l >> 4) & 3) << 1)) * 16 + 128 * j;
      for (u32 n = w - 1; n < 56; n += 3) dma<0, false>(L_WO + n * 1024, vo, sWO, n * 8 * 896);
      const u32 vz = vrow8 * 3584 + (vpos ^ (((l >> 4) & 1) << 2)) * 16 + 128 * g14 + 1792;
      for (u32 n = w - 1; n < 56; n += 3) dma<0, false>(L_WZ2 + n * 1024, vz, sWIN, n * 8 * 3584);
    }
  } else {
    poll1(ws, flag_ptr(ws, F_KVP, (l & 31) * 8 + j), l < 32, tag);
    STAMP(5);
    u32x4 pr[8];
    const u32 vo = WS_KVP + (j * 32 + p1) * 8192 + l * 16;
    SFOR(i, 0, 4) ld128<i * 1024, true>(pr[i], vo, sWS, zero); SEND
    SFOR(i, 0, 4) ld128<i * 1024, true>(pr[4 + i], vo, sWS, 4096); SEND
    waitvm<0>();
    SFOR(i, 0, 8) pin(pr[i]); SEND
    f32x4 s = __builtin_bit_cast(f32x4, pr[0]);
    SFOR(i, 1, 8) s += __builtin_bit_cast(f32x4, pr[i]); SEND
    SFOR(e, 0, 4) s[e] = add_x32(add_x16(s[e])); SEND
    const u32 a = cvtpk(s[0], s[1]), b = cvtpk(s[2], s[3]);
    const u32 a2 = __builtin_amdgcn_update_dpp(0, (int)a, 0x4E, 0xf, 0xf, true), b2 = __builtin_amdgcn_update_dpp(0, (int)b, 0x4E, 0xf, 0xf, true);
    const u32 qd = l & 7, g = 2 * (qd >> 2) + (qd & 1), i = p1 >> 1, rq = (i & 7) >> 1;
    const u32 pos = 2 * (g ^ ((4 - rq) & 3)) + ((l >> 3) & 1);
    if (l < 16 && !(qd & 2)) st128(rWS, WS_KVR + ((2 * j + (p1 & 1)) * 16 + i) * 128 + pos * 16, u32x4{a, b, a2, b2});
    waitvm<0>();
    if (l == 0) set_flag(ws, F_KVR, id, tag);
    STAMP(6);
  }
  if (j == 7) {
    if (p1 < 28) {
      u32x4 wr[14];
      const u32 vo = (112 * w + vrow8) * 3584 + vpos * 16 + 128 * p1;
      SFOR(i, 0, 14) ld128<0, false>(wr[i], vo, sWIN, i * 8 * 3584); SEND
      waitvm<0>();
      f32x2 c[4] = {};
      SFOR(i, 0, 14) pin(wr[i]); SFOR(e, 0, 4) c[e] += f32x2{bflo(wr[i][e]), bfhi(wr[i][e])}; SEND SEND
      float cs[8];
      SFOR(e, 0, 8) cs[e] = add_x32(add_x16(dpp_add<0x128>(c[e >> 1][e & 1]))); SEND
      if (l < 8) {
        *(LAS f32x4*)(L_MISC + w * 256 + l * 32) = f32x4{cs[0], cs[1], cs[2], cs[3]};
        *(LAS f32x4*)(L_MISC + w * 256 + l * 32 + 16) = f32x4{cs[4], cs[5], cs[6], cs[7]};
      }
      bar();
      if (w == 0) {
        if (l < 16) {
          const u32 a = L_MISC + l * 16;
          const f32x4 t = (lds128f(a) + lds128f(a + 256)) + (lds128f(a + 512) + lds128f(a + 768));
          st128(rWS, WS_CS + p1 * 256 + l * 16, __builtin_bit_cast(u32x4, t));
        }
        waitvm<0>();
        if (l == 0) set_flag(ws, F_CS, p1, tag);
      }
    }
    TSOUT();
    return;
  }
  if (w == 0) {
    poll1(ws, flag_ptr(ws, F_PQ, p1 * 8 + (l & 7)), l < 8, tag);
    poll4(ws, flag_ptr(ws, F_KVR, l), tag);
    if (wg == 0 && l == 0) __hip_atomic_store((GAS u64*)(ws + WS_EPOCH), tag, RLX_AGENT);
  }
  bar();
  STAMP(7);
  const u32 t16 = l & 15, g4 = l >> 4;
  f32x4 hacc[4];
  {
    u32x4 kr[8], pq[16];
    u32x2 xt[4];
    const u32 vk = WS_KVR + w * 8192 + l * 16;
    SFOR(i, 0, 4) ld128<i * 1024, true>(kr[i], vk, sWS, zero); SEND
    SFOR(i, 0, 4) ld128<i * 1024, true>(kr[4 + i], vk, sWS, 4096); SEND
    const u32 vp = WS_PQ + (p1 * 4 + w) * 16384 + l * 16;
    SFOR(i, 0, 16) ld128<(i & 3) * 1024, true>(pq[i], vp, sWS, (i >> 2) * 4096); SEND
    const u32 vx = (64 * p1 + 16 * w + t16) * 896 + 128 * j + 8 * g4;
    SFOR(i, 0, 4) ld64<i * 32, false>(xt[i], vx, sX, zero); SEND
    waitvm<20>();
    SFOR(i, 0, 8) pin(kr[i]); *(LAS u32x4*)(L_KVR + w * 8192 + i * 1024 + l * 16) = kr[i]; SEND
    bar();
    STAMP(8);
    const u32 aK = L_KVR + (t16 >> 1) * 128 + (2 * (g4 ^ ((4 - (t16 >> 2)) & 3)) + (t16 & 1)) * 16;
    u32 aO[2][4];
    {
      const u32 r0 = 4 * g4 + trq, r1 = 16 + 3 * g4 + (trq < 2 ? trq : 2);
      SFOR(y, 0, 4)
        aO[0][y] = L_WO + r0 * 128 + ((y ^ ((r0 >> 1) & 3)) * 32) + 8 * trp;
        aO[1][y] = L_WO + r1 * 128 + ((y ^ ((r1 >> 1) & 3)) * 32) + 8 * trp;
      SEND
    }
    SFOR(n, 0, 4) hacc[n] = f32x4{0.f, 0.f, 0.f, 0.f}; SEND
    SFOR(h, 0, 16)
      waitpin<19 - h>(pq[h]);
      const f32x4 z4 = {0.f, 0.f, 0.f, 0.f};
      const f32x4 d0 = mfma16(lds128(aK + h * 2048), pq[h], z4), d1 = mfma16(lds128(aK + h * 2048 + 1024), pq[h], z4);
      const float r = __builtin_amdgcn_rcpf(d1[3] + 1e-6f);
      const u32x4 ob = {cvtpk(d0[0] * r, d0[1] * r), cvtpk(d0[2] * r, d0[3] * r), cvtpk(d1[0] * r, d1[1] * r), cvtpk(d1[2] * r, 0.f)};
      SFOR(n, 0, 4)
        constexpr int y = n ^ (2 * (h & 1));
        hacc[n] = mfma16(trfrag(aO[0][y] + h * 3584, aO[1][y] + h * 3584), ob, hacc[n]);
      SEND
    SEND
    waitvm<0>();
    float s = 0.f, q = 0.f;
    SFOR(n, 0, 4)
      pin(xt[n]);
      hacc[n] += f32x4{bflo(xt[n].x), bfhi(xt[n].x), bflo(xt[n].y), bfhi(xt[n].y)};
      SFOR(e, 0, 4) s += hacc[n][e]; q = __builtin_fmaf(hacc[n][e], hacc[n][e], q); SEND
    SEND
    const u32 o = WS_H + ((2 * p1 + (w >> 1)) * 28 + 4 * j + (g4 >> 1)) * 1024 + (g4 & 1) * 512 + (16 * (w & 1) + t16) * 16;
    SFOR(m, 0, 2)
      st128(rWS, o + m * 2048, u32x4{cvtpk(hacc[2 * m][0], hacc[2 * m][1]), cvtpk(hacc[2 * m][2], hacc[2 * m][3]), cvtpk(hacc[2 * m + 1][0], hacc[2 * m + 1][1]),
                                     cvtpk(hacc[2 * m + 1][2], hacc[2 * m + 1][3])});
    SEND
    s = add_x32(add_x16(s));
    q = add_x32(add_x16(q));
    if (l < 16) st64(rWS, WS_HST + ((P * 7 + j) * 128 + 64 * (p1 & 1) + 16 * w + l) * 8, u32x2{asu(s), asu(q)});
  }
  waitvm<0>();
  bar();
  if (tid == 0) set_flag(ws, F_H, id, tag);
  STAMP(9);
  if (w != 0) {
    const u32 vz = vrow8 * 3584 + (vpos ^ (((l >> 4) & 1) << 2)) * 16 + 128 * g14;
    for (u32 n = w - 1; n < 56; n += 3) dma<0, false>(L_WZ1 + n * 1024, vz, sWIN, n * 8 * 3584);
  } else {
    const u32 m = l < 14 ? l : 0, pp = 2 * P + (m >= 7), jj = m >= 7 ? m - 7 : m;
    GAS u64* fp = l < 14 ? flag_ptr(ws, F_H, pp * 8 + jj) : flag_ptr(ws, F_CS, g14 + (l & 1) * 14);
    poll1(ws, fp, l < 16, tag);
  }
  bar();
  STAMP(10);
  const u32 t32 = l & 31;
  const u32x4 sWSP = make_srd(wsp_, 2048 * 4096);
  u32 vS[2];
  SFOR(i, 0, 2) vS[i] = (128 * P + 32 * w + vrow8) * 4096 + (vpos ^ (4 * i + (l >> 4))) * 16; SEND
  const u32 spA = w * 8192, spB = w * 12288;
  auto sp_issue = [&](auto c_, auto s_) __attribute__((always_inline)) {
    constexpr int c = decltype(c_)::value, s = decltype(s_)::value;
    const u32 kb = ((P + (c >> 1)) & 15) * 256 + (c & 1) * 128;
    SFOR(i, 0, 4) dma<0, false>(sp_slot(s) + (s < 2 ? spA : spB) + i * 1024, vS[i & 1], sWSP, kb + i * 8 * 4096); SEND
  };
  u32 aS[2][4];
  SFOR(k, 0, 4)
    const u32 b = t32 * 128 + (((2 * k + hf) ^ ((t32 >> 1) & 7)) * 16);
    aS[0][k] = L_SPA + spA + b;
    aS[1][k] = L_SPB + spB + b;
  SEND
  u32x4 res[NRES * 8];
  auto sp_take = [&](auto c_, auto s_) __attribute__((always_inline)) {
    constexpr int c = decltype(c_)::value, s = decltype(s_)::value, o = (s < 2 ? s : s - 2) * 4096, r = (c - 2 * NSTREAM) * 4;
    take4<o, (r < 48)>(res[r], res[r + 1], res[r + 2], res[r + 3], aS[s >= 2][0], aS[s >= 2][1], aS[s >= 2][2], aS[s >= 2][3]);
  };
#define IC(v) std::integral_constant<int, (v)>{}
  u32 z1p[16];
  u32 bbits;
  {
    u32x4 hb[28];
    u32x2 st[4];
    u32x4 cs[8];
    const u32 vst = WS_HST + ((P * 7 + 4 * hf) * 128 + 32 * w + t32) * 8;
    SFOR(i, 0, 3) ld64<i * 1024, true>(st[i], vst, sWS, zero); SEND
    ld64<0, true>(st[3], vst + (hf ? 0 : 3072), sWS, zero);
    const u32 vh = WS_H + (4 * P + w) * 28 * 1024 + l * 16;
    SFOR(i, 0, 28) ld128<(i & 3) * 1024, true>(hb[i], vh, sWS, (i >> 2) * 4096); SEND
    const u32 vc2 = WS_CS + (896 + 64 * g14 + 4 * hf) * 4;
    SFOR(i, 0, 8) ld128<i * 32, true>(cs[i], vc2, sWS, zero); SEND
    {
      const u32x4 sB = make_srd(bsp_, 4096);
      asm volatile("buffer_load_ushort %0, %1, %2, 0 offen" : "=v"(bbits) : "v"((128 * P + 32 * w + t32) * 2), "s"(sB) : "memory");
    }
    const u32 rowp = (4 * hf + trq) * 128 + blk * 32;
    const u32 aZ2[2] = {L_WZ2 + rowp + ((trq >> 1) * 64) + 8 * trp, L_WZ2 + rowp + (((trq >> 1) ^ 1) * 64) + 8 * trp};
    const u32 pp = 8 * (2 * (trp & 1) + (trp >> 1));
    const u32 aZ1[2] = {L_WZ1 + rowp + ((trq >> 1) * 64) + pp, L_WZ1 + rowp + (((trq >> 1) ^ 1) * 64) + pp};
    f32x16 z[2] = {};
    SFOR(k, 0, 28)
      if constexpr ((k & 1) == 0) { waitpin<9 + 26 - k>(hb[k]); pin(hb[k + 1]); }
      constexpr int ro = (32 * (k >> 1) + 8 * (k & 1)) * 128;
      SFOR(f, 0, 2) z[f] = mfma32(trfrag(aZ2[f] + ro, aZ2[f] + ro + 2048), hb[k], z[f]); SEND
    SEND
    waitvm<0>();
    STAMP(11);
    SFOR(i, 0, 4) pin(st[i]); SEND
    SFOR(i, 0, 8) pin(cs[i]); SEND
    pin(bbits);
    float rs, nm;
    {
      float s = asf(st[0].x) + asf(st[1].x) + asf(st[2].x);
      float q = asf(st[0].y) + asf(st[1].y) + asf(st[2].y);
      s += hf ? 0.f : asf(st[3].x);
      q += hf ? 0.f : asf(st[3].y);
      s = add_x32(s) * (1.0f / 448);
      q = add_x32(q) * (1.0f / 448);
      rs = __builtin_amdgcn_rsqf(__builtin_fmaxf(q - s * s, 0.f) + 1e-5f);
      nm = -s * rs;
    }
    f32x2 zz[16];
    {
      f32x2 s2 = {0.f, 0.f}, q2 = {0.f, 0.f};
      SFOR(i, 0, 16)
        constexpr int f = i >> 3, e = (2 * i) & 15;
        const f32x4 c = __builtin_bit_cast(f32x4, cs[i >> 1]);
        const f32x2 u = __builtin_elementwise_fma(f32x2{c[(2 * i) & 3], c[((2 * i) & 3) + 1]}, f32x2{nm, nm}, f32x2{z[f][e], z[f][e + 1]} * f32x2{rs, rs});
        zz[i] = gelu2(u);
        s2 += zz[i];
        q2 = __builtin_elementwise_fma(zz[i], zz[i], q2);
      SEND
      const float s = add_x32(s2.x + s2.y), q = add_x32(q2.x + q2.y);
      if (l < 32) st64(rWS, WS_ZST + ((P * 14 + g14) * 128 + 32 * w + l) * 8, u32x2{asu(s), asu(q)});
    }
    waitvm<0>();
    bar();
    if (tid == 0) set_flag(ws, F_ZS, id, tag);
    STAMP(12);
    const u32 vc1 = WS_CS + (64 * g14 + 8 * hf) * 4;
    SFOR(i, 0, 8) ld128<(i >> 2) * 128 + ((i >> 1) & 1) * 64 + (i & 1) * 16, true>(cs[i], vc1, sWS, zero); SEND
    SFOR(s, 0, 5) sp_issue(IC(2 * NSTREAM + s), IC(s)); SEND
    z[0] = f32x16{};
    z[1] = f32x16{};
    SFOR(k, 0, 28)
      constexpr int ro = (32 * (k >> 1) + 8 * (k & 1)) * 128;
      SFOR(f, 0, 2) z[f] = mfma32(trfrag(aZ1[f] + ro, aZ1[f] + ro + 2048), hb[k], z[f]); SEND
      if constexpr (k >= 8 && (k & 3) == 0) {
        constexpr int s = (k - 8) >> 2;
        waitvm<16>();
        sp_take(IC(2 * NSTREAM + s), IC(s));
        sp_issue(IC(2 * NSTREAM + 5 + s), IC(s));
      }
    SEND
    STAMP(13);
    if (w == 0) {
      const u32 m = l < 14 ? l : 0, pp2 = 2 * P + (m >= 7), jj = m >= 7 ? m - 7 : m;
      poll1(ws, flag_ptr(ws, F_ZS, pp2 * 8 + jj), l < 14, tag);
    }
    bar();
    STAMP(14);
    {
      u32x2 zs[7];
      const u32 vz = WS_ZST + ((P * 14 + 7 * hf) * 128 + 32 * w + t32) * 8;
      SFOR(i, 0, 7) ld64<(i & 3) * 1024, true>(zs[i], vz, sWS, (i >> 2) * 4096); SEND
      waitvm<0>();
      float s = 0.f, q = 0.f;
      SFOR(i, 0, 7) pin(zs[i]); s += asf(zs[i].x); q += asf(zs[i].y); SEND
      s = add_x32(s) * (1.0f / 896);
      q = add_x32(q) * (1.0f / 896);
      const float r2 = __builtin_amdgcn_rsqf(__builtin_fmaxf(q - s * s, 0.f) + 1e-5f), n2 = -s * r2;
      const u32 o = WS_Z + (g14 * 64 + 4 * P + w) * 4096 + hf * 512;
      SFOR(c, 0, 4)
        u32x4 v;
        SFOR(e, 0, 4) const f32x2 t = __builtin_elementwise_fma(zz[4 * c + e], f32x2{r2, r2}, f32x2{n2, n2}); v[e] = cvtpk(t.x, t.y); SEND
        st128(rWS, o + c * 1024 + (t32 ^ ((2 * (c & 1) + hf) << 2)) * 16, v);
      SEND
    }
    waitvm<0>();
    bar();
    if (tid == 0) set_flag(ws, F_Z, id, tag);
    STAMP(15);
    SFOR(s, 0, 5) sp_take(IC(2 * NSTREAM + 5 + s), IC(s)); if constexpr (10 + s < 2 * NRES) sp_issue(IC(2 * NSTREAM + 10 + s), IC(s)); SEND
    SFOR(i, 0, 8) pin(cs[i]); SEND
    SFOR(i, 0, 16)
      constexpr int f = i >> 3, e = (2 * i) & 15;
      const f32x4 c = __builtin_bit_cast(f32x4, cs[i >> 1]);
      const f32x2 u = __builtin_elementwise_fma(f32x2{c[(2 * i) & 3], c[((2 * i) & 3) + 1]}, f32x2{nm, nm}, f32x2{z[f][e], z[f][e + 1]} * f32x2{rs, rs});
      const f32x2 t = gelu2(u);
      z1p[i] = cvtpk(t.x, t.y);
    SEND
  }
  static_assert(NRES == 10, "the schedule below is written for 20 resident chunks");
  SFOR(s, 0, 5) waitvm<16>(); sp_take(IC(2 * NSTREAM + 10 + s), IC(s)); sp_issue(IC(2 * NSTREAM + 15 + s), IC(s)); SEND
  SFOR(s, 0, 5) waitvm<16>(); sp_take(IC(2 * NSTREAM + 15 + s), IC(s)); sp_issue(IC(s), IC(s)); SEND
  STAMP(16);
  if (w == 0) poll1(ws, flag_ptr(ws, F_Z, (2 * (l & 15) + (p1 & 1)) * 8 + j), l < 16, tag);
  bar();
  STAMP(17);
  {
    const u32 vZ = WS_Z + g14 * 64 * 4096 + w * 4096 + l * 16;
    auto z_issue = [&](auto st_) __attribute__((always_inline)) {
      constexpr int st = decltype(st_)::value;
      const u32 so = ((P + st) & 15) * 16384, la = L_ZR + (st & 3) * 16384 + w * 4096;
      SFOR(i, 0, 4) dma<i * 1024, true>(la, vZ, sWS, so); SEND
    };
    SFOR(st, 0, 3) z_issue(IC(st)); SEND
    u32 aZ[4];
    {
      const u32 o3 = 2 * blk + (trp >> 1), b0 = L_ZR + o3 * 512 + (((2 * hf) ^ o3) * 4 + trq) * 16 + (trp & 1) * 8;
      SFOR(v, 0, 4) aZ[v] = b0 ^ (64 * (v & 1)) ^ (256 * (v >> 1)); SEND
    }
    f32x16 acc[2] = {};
    SFOR(st, 0, 16)
      constexpr int spc = (st >= 1 && 2 * (st - 1) + 5 < 2 * NSTREAM ? 1 : 0) + (st >= 1 && 2 * (st - 1) + 6 < 2 * NSTREAM ? 1 : 0)
                        + (st >= 2 && 2 * (st - 2) + 5 < 2 * NSTREAM ? 1 : 0) + (st >= 2 && 2 * (st - 2) + 6 < 2 * NSTREAM ? 1 : 0)
                        + (st >= 3 && 2 * (st - 3) + 5 < 2 * NSTREAM ? 1 : 0) + (st >= 3 && 2 * (st - 3) + 6 < 2 * NSTREAM ? 1 : 0);
      constexpr int zl = (st + 1 < 16 ? 1 : 0) + (st + 2 < 16 ? 1 : 0);
      waitvm<4 * (zl + spc)>();
      bar();
      if constexpr (st + 3 < 16) z_issue(IC(st + 3));
      SFOR(cc, 0, 2)
        constexpr int c = 2 * st + cc;
        u32x4 bs[4];
        if constexpr (st < NSTREAM) {
          constexpr int s = c % 5;
          if constexpr (c >= 5) {
            constexpr int ist = (c - 5) >> 1;
            constexpr int zy = (st - ist) - ((st + 3 < 16) ? 0 : 0);
            constexpr int cy = (c + 4 < 2 * NSTREAM ? 4 : 2 * NSTREAM - 1 - c);
            waitvm<4 * (zy + cy)>();
          }
          SFOR(k, 0, 4) bs[k] = lds128(aS[s >= 2][k] + (s < 2 ? s : s - 2) * 4096); SEND
          if constexpr (c + 5 < 2 * NSTREAM) {
            asm volatile("s_waitcnt lgkmcnt(0)" : "+v"(bs[0]), "+v"(bs[1]), "+v"(bs[2]), "+v"(bs[3])::"memory");
            sp_issue(IC(c + 5), IC(s));
          }
        }
        SFOR(k, 0, 4)
          constexpr int ks = 4 * cc + k, off = (st & 3) * 16384 + (ks >> 1) * 4096;
          SFOR(f, 0, 2)
            const u32x4 a = trfrag(aZ[2 * (ks & 1)] + off + f * 2048, aZ[2 * (ks & 1) + 1] + off + f * 2048);
            if constexpr (st < NSTREAM) acc[f] = mfma32(a, bs[k], acc[f]);
            else acc[f] = mfma32(a, res[(c - 2 * NSTREAM) * 4 + k], acc[f]);
          SEND
        SEND
      SEND
    SEND
    STAMP(18);
    const float b = bflo(bbits);
    const u32 o = WS_Y + ((8 * P + 2 * w + blk) * 28 + 2 * g14) * 1024 + hf * 256 + (l & 15) * 16;
    SFOR(c, 0, 4)
      u32x4 v;
      SFOR(e, 0, 4)
        constexpr int i = 8 * (c & 1) + 2 * e;
        const u32 zp = z1p[4 * c + e];
        v[e] = cvtpk(bflo(zp) * (acc[c >> 1][i] + b), bfhi(zp) * (acc[c >> 1][i + 1] + b));
      SEND
      st128(rWS, o + (c >> 1) * 1024 + (c & 1) * 512, v);
    SEND
  }
  waitvm<0>();
  bar();
  if (tid == 0) set_flag(ws, F_Y, id, tag);
  STAMP(19);
  if (w != 0) {
    const u32x4 sWOUT = make_srd(wout_, 896 * 896);
    const u32 x0 = (l >> 4) & 1;
    const u32 vo0 = vrow8 * 896 + (vpos ^ (x0 << 1)) * 16 + 128 * j, vo1 = vrow8 * 896 + (vpos ^ ((x0 | 2) << 1)) * 16 + 128 * j;
    for (u32 n = w - 1; n < 112; n += 3) dma<0, false>(L_WOUT + n * 1024, (n & 1) ? vo1 : vo0, sWOUT, n * 8 * 896);
  } else {
    const u32 m = l < 14 ? l : 0, pp = 2 * P + (m >= 7), jj = m >= 7 ? m - 7 : m;
    poll1(ws, flag_ptr(ws, F_Y, pp * 8 + jj), l < 14, tag);
  }
  bar();
  STAMP(20);
  {
    u32x4 yb[28];
    const u32 vy = WS_Y + (4 * p1 + w) * 28 * 1024 + l * 16;
    SFOR(i, 0, 28) ld128<(i & 3) * 1024, true>(yb[i], vy, sWS, (i >> 2) * 4096); SEND
    waitvm<28>();
    bar();
    STAMP(21);
    u32 aW[4];
    {
      const u32 xs = (trq >> 1) | ((g4 & 1) << 1);
      SFOR(n, 0, 4) aW[n] = L_WOUT + (8 * g4 + trq) * 128 + ((n ^ xs) * 32) + 8 * trp; SEND
    }
    SFOR(k, 0, 28)
      if constexpr ((k & 1) == 0) { waitpin<26 - k>(yb[k]); pin(yb[k + 1]); }
      SFOR(n, 0, 4) hacc[n] = mfma16(trfrag(aW[n] + k * 4096, aW[n] + k * 4096 + 512), yb[k], hacc[n]); SEND
    SEND
    STAMP(22);
    const __amdgpu_buffer_rsrc_t rO = __builtin_amdgcn_make_buffer_rsrc(out_, 0, 2048 * 896, 0x00020000);
    const u32 o = (64 * p1 + 16 * w + t16) * 896 + 128 * j + (g4 & 1) * 32 + (g4 >> 1) * 16;
    SFOR(m, 0, 2)
      const auto r0 = __builtin_amdgcn_permlane16_swap(cvtpk(hacc[2 * m][0], hacc[2 * m][1]), cvtpk(hacc[2 * m + 1][0], hacc[2 * m + 1][1]), false, false);
      const auto r1 = __builtin_amdgcn_permlane16_swap(cvtpk(hacc[2 * m][2], hacc[2 * m][3]), cvtpk(hacc[2 * m + 1][2], hacc[2 * m + 1][3]), false, false);
      __builtin_amdgcn_raw_buffer_store_b128(u32x4{(u32)r0[0], (u32)r1[0], (u32)r0[1], (u32)r1[1]}, rO, o + m * 64, 0, 0);
    SEND
  }
  STAMP(23);
  TSOUT();
}

__global__ void k_pre(char* ws) { if (blockIdx.x == 0 && threadIdx.x == 0) *(u32*)(ws + WS_DBG + 65536) = (u32)__builtin_amdgcn_s_memrealtime(); }
__global__ void k_post(char* ws, int amp) {
  const u32 now = (u32)__builtin_amdgcn_s_memrealtime();
  const u32 wgi = threadIdx.x; const u32 xcd = wgi & 7, rr_ = wgi >> 3, j = 4 * (xcd & 1) + (rr_ >> 3);
  u32 e = ((u32*)(ws + WS_DBG))[wgi * 32 + 23];
  u32 d = j == 7 ? 0xffffffffu : now - e;
  __shared__ u32 m; if (threadIdx.x == 0) m = 0xffffffffu; __syncthreads(); atomicMin(&m, d); __syncthreads();
  const u32 until = now + amp * m; while ((int)((u32)__builtin_amdgcn_s_memrealtime() - until) < 0) __builtin_amdgcn_s_sleep(2);
}
}

extern "C" void kernel_launch(void* const* d_in, const int* in_sizes, int n_in, void* d_out, int out_size, void* d_ws, size_t ws_size, hipStream_t stream) {
  fused_kernel<<<256, 256, 0, stream>>>(d_in[0], d_in[1], d_in[2], d_in[4], d_in[5], d_in[6], d_in[7], d_out, (char*)d_ws);
}
```

```cpp
#define TS
#define AMP 0
#include <hip/hip_runtime.h>
#include <hip/hip_bf16.h>
#include <stdint.h>
#include <type_traits>
#pragma clang diagnostic ignored "-Wint-to-pointer-cast"

namespace {

#define DI __device__ __forceinline__
#define LAS __attribute__((address_space(3)))
#define GAS __attribute__((address_space(1)))
typedef unsigned u32;
typedef unsigned long long u64;
typedef u32 u32x2 __attribute__((ext_vector_type(2)));
typedef u32 u32x4 __attribute__((ext_vector_type(4)));
typedef float f32x2 __attribute__((ext_vector_type(2)));
typedef float f32x4 __attribute__((ext_vector_type(4)));
typedef float f32x16 __attribute__((ext_vector_type(16)));
typedef __bf16 bf16x8 __attribute__((ext_vector_type(8)));
typedef __bf16 bf16x2 __attribute__((ext_vector_type(2)));
typedef short s16x4 __attribute__((ext_vector_type(4)));
#define RLX_AGENT __ATOMIC_RELAXED, __HIP_MEMORY_SCOPE_AGENT

constexpr u32 WS_EPOCH = 0, WS_TMO = 128;
constexpr u32 WS_FLAGS = 4096;
constexpr u32 F_KVP = 0, F_KVR = 1, F_PQ = 2, F_H = 3, F_ZS = 4, F_Z = 5, F_Y = 6, F_CS = 7;
constexpr u32 WS_KVP = 1u << 20;
constexpr u32 WS_KVR = WS_KVP + (2u << 20);
constexpr u32 WS_CS = WS_KVR + 32768;
constexpr u32 WS_HST = WS_CS + 8192;
constexpr u32 WS_ZST = WS_HST + 16 * 7 * 128 * 8;
constexpr u32 WS_PQ = 4u << 20;
constexpr u32 WS_H = 6u << 20;
constexpr u32 WS_Z = 8u << 20;
constexpr u32 WS_Y = 12u << 20;
constexpr u32 WS_DBG = 16u << 20;
constexpr u32 WS_BYTES = 17u << 20;
static_assert(WS_ZST + 16 * 14 * 128 * 8 <= WS_PQ, "ws map");

constexpr u32 L_X = 0, XSTR = 912;
constexpr u32 L_WKV = 64 * XSTR;
constexpr u32 L_WQ = L_WKV;
constexpr u32 L_KST = L_WKV + 53248;
constexpr u32 L_WO = 0;
constexpr u32 L_WZ2 = 57344;
constexpr u32 L_WZ1 = 0;
constexpr u32 L_KVR = 114688;
constexpr u32 L_ZR = 0;
constexpr u32 L_SPA = 114688, L_SPB = 65536;
constexpr u32 L_WOUT = 0;
constexpr u32 L_MISC = 160768;
constexpr u32 LDS_BYTES = 163840;

constexpr u32 SPIN_CAP = 1u << 16;
constexpr float LOG2E = 1.4426950408889634f;

DI u32 rfl(u32 x) { return __builtin_amdgcn_readfirstlane(x); }
DI u32x4 make_srd(const void* p, u32 bytes) {
  const u64 a = (u64)p;
  return u32x4{(u32)a, (u32)(a >> 32) & 0xffffu, bytes, 0x00020000u};
}
DI u32 cvtpk(float lo, float hi) {
  f32x2 v = {lo, hi};
  return __builtin_bit_cast(u32, __builtin_convertvector(v, bf16x2));
}
DI float asf(u32 x) { return __builtin_bit_cast(float, x); }
DI u32 asu(float x) { return __builtin_bit_cast(u32, x); }
DI float bflo(u32 x) { return __builtin_bit_cast(float, x << 16); }
DI float bfhi(u32 x) { return __builtin_bit_cast(float, x & 0xffff0000u); }
DI f32x16 mfma32(u32x4 a, u32x4 b, f32x16 c) {
  return __builtin_amdgcn_mfma_f32_32x32x16_bf16(__builtin_bit_cast(bf16x8, a), __builtin_bit_cast(bf16x8, b), c, 0, 0, 0);
}
DI f32x4 mfma16(u32x4 a, u32x4 b, f32x4 c) {
  return __builtin_amdgcn_mfma_f32_16x16x32_bf16(__builtin_bit_cast(bf16x8, a), __builtin_bit_cast(bf16x8, b), c, 0, 0, 0);
}
DI u32x2 tr64(u32 a) { return __builtin_bit_cast(u32x2, __builtin_amdgcn_ds_read_tr16_b64_v4i16((LAS s16x4*)a)); }
DI u32x4 trfrag(u32 a0, u32 a1) {
  const u32x2 lo = tr64(a0), hi = tr64(a1);
  return u32x4{lo.x, lo.y, hi.x, hi.y};
}
DI u32x4 lds128(u32 a) { return *(LAS u32x4*)a; }
DI f32x4 lds128f(u32 a) { return *(LAS f32x4*)a; }
DI void bar() { asm volatile("s_waitcnt lgkmcnt(0)\n\ts_barrier" ::: "memory"); }
template <int N> DI void waitvm() { asm volatile("s_waitcnt vmcnt(%0)" ::"i"(N) : "memory"); }
template <int IMM, bool SC1> DI void dma(u32 ldsaddr, u32 voff, u32x4 srd, u32 soff) {
  if constexpr (SC1)
    asm volatile("s_mov_b32 m0, %0\n\ts_nop 0\n\tbuffer_load_dwordx4 %1, %2, %3 offen offset:%4 sc1 lds" ::"s"(ldsaddr), "v"(voff), "s"(srd), "s"(soff), "i"(IMM) : "memory");
  else
    asm volatile("s_mov_b32 m0, %0\n\ts_nop 0\n\tbuffer_load_dwordx4 %1, %2, %3 offen offset:%4 lds" ::"s"(ldsaddr), "v"(voff), "s"(srd), "s"(soff), "i"(IMM) : "memory");
}
template <int IMM, bool SC1> DI void ld128(u32x4& d, u32 voff, u32x4 srd, u32 soff) {
  if constexpr (SC1) asm volatile("buffer_load_dwordx4 %0, %1, %2, %3 offen offset:%4 sc1" : "=v"(d) : "v"(voff), "s"(srd), "s"(soff), "i"(IMM) : "memory");
  else asm volatile("buffer_load_dwordx4 %0, %1, %2, %3 offen offset:%4" : "=v"(d) : "v"(voff), "s"(srd), "s"(soff), "i"(IMM) : "memory");
}
template <int IMM, bool SC1> DI void ld64(u32x2& d, u32 voff, u32x4 srd, u32 soff) {
  if constexpr (SC1) asm volatile("buffer_load_dwordx2 %0, %1, %2, %3 offen offset:%4 sc1" : "=v"(d) : "v"(voff), "s"(srd), "s"(soff), "i"(IMM) : "memory");
  else asm volatile("buffer_load_dwordx2 %0, %1, %2, %3 offen offset:%4" : "=v"(d) : "v"(voff), "s"(srd), "s"(soff), "i"(IMM) : "memory");
}
template <int N, typename T> DI void waitpin(T& a) { asm volatile("s_waitcnt vmcnt(%1)" : "+v"(a) : "i"(N) : "memory"); }
template <typename T> DI void pin(T& a) { asm volatile("" : "+v"(a)); }
template <int OFF, bool ACC> DI void take4(u32x4& r0, u32x4& r1, u32x4& r2, u32x4& r3, u32 a0, u32 a1, u32 a2, u32 a3) {
  if constexpr (ACC)
    asm volatile("ds_read_b128 %0, %4 offset:%8\n\tds_read_b128 %1, %5 offset:%8\n\tds_read_b128 %2, %6 offset:%8\n\tds_read_b128 %3, %7 offset:%8\n\ts_waitcnt lgkmcnt(0)"
                 : "=&a"(r0), "=&a"(r1), "=&a"(r2), "=&a"(r3) : "v"(a0), "v"(a1), "v"(a2), "v"(a3), "i"(OFF) : "memory");
  else
    asm volatile("ds_read_b128 %0, %4 offset:%8\n\tds_read_b128 %1, %5 offset:%8\n\tds_read_b128 %2, %6 offset:%8\n\tds_read_b128 %3, %7 offset:%8\n\ts_waitcnt lgkmcnt(0)"
                 : "=&v"(r0), "=&v"(r1), "=&v"(r2), "=&v"(r3) : "v"(a0), "v"(a1), "v"(a2), "v"(a3), "i"(OFF) : "memory");
}
DI void st128(__amdgpu_buffer_rsrc_t rs, u32 off, u32x4 v) { __builtin_amdgcn_raw_buffer_store_b128(v, rs, off, 0, 16); }
DI void st64(__amdgpu_buffer_rsrc_t rs, u32 off, u32x2 v) { __builtin_amdgcn_raw_buffer_store_b64(v, rs, off, 0, 16); }
template <int CTRL> DI float dpp_add(float x) {
  return x + __builtin_bit_cast(float, __builtin_amdgcn_update_dpp(0, __builtin_bit_cast(int, x), CTRL, 0xf, 0xf, true));
}
DI float add_x16(float x) {
  const auto r = __builtin_amdgcn_permlane16_swap(__builtin_bit_cast(u32, x), __builtin_bit_cast(u32, x), false, false);
  return __builtin_bit_cast(float, (u32)r[0]) + __builtin_bit_cast(float, (u32)r[1]);
}
DI float add_x32(float x) {
  const auto r = __builtin_amdgcn_permlane32_swap(__builtin_bit_cast(u32, x), __builtin_bit_cast(u32, x), false, false);
  return __builtin_bit_cast(float, (u32)r[0]) + __builtin_bit_cast(float, (u32)r[1]);
}
DI float phi(float t) { return (t > 0.f ? t + 1.0f : __builtin_amdgcn_exp2f(t * LOG2E)) + 1e-6f; }
DI f32x2 gelu2(f32x2 x) {
  const float c1 = -2.0f * 0.7978845608028654f * LOG2E, c3 = c1 * 0.044715f;
  const f32x2 p = __builtin_elementwise_fma(x * x, f32x2{c3, c3}, f32x2{c1, c1}) * x;
  const f32x2 e = f32x2{__builtin_amdgcn_exp2f(p.x), __builtin_amdgcn_exp2f(p.y)} + f32x2{1.f, 1.f};
  return x * f32x2{__builtin_amdgcn_rcpf(e.x), __builtin_amdgcn_rcpf(e.y)};
}

DI GAS u64* flag_ptr(char* ws, u32 arr, u32 idx) { return (GAS u64*)(ws + WS_FLAGS + (arr * 256 + idx) * 128); }
DI void set_flag(char* ws, u32 arr, u32 idx, u64 tag) { __hip_atomic_store(flag_ptr(ws, arr, idx), tag, RLX_AGENT); }
DI void poll1(char* ws, GAS u64* p, bool on, u64 tag) {
  for (u32 spins = 0;; ++spins) {
    u64 v = tag;
    if (on) v = __hip_atomic_load(p, RLX_AGENT);
    if (__all(v == tag)) break;
    if ((spins & 63u) == 63u) {
      if (spins > SPIN_CAP) __hip_atomic_store((GAS u64*)(ws + WS_TMO), tag, RLX_AGENT);
      if (__hip_atomic_load((GAS u64*)(ws + WS_TMO), RLX_AGENT) == tag) break;
    }
  }
  __builtin_amdgcn_fence(__ATOMIC_ACQUIRE, "wavefront");
}
DI void poll4(char* ws, GAS u64* p, u64 tag) {
  for (u32 spins = 0;; ++spins) {
    const u64 a = __hip_atomic_load(p, RLX_AGENT), b = __hip_atomic_load(p + 64 * 16, RLX_AGENT), c = __hip_atomic_load(p + 128 * 16, RLX_AGENT),
              d = __hip_atomic_load(p + 192 * 16, RLX_AGENT);
    if (__all(a == tag && b == tag && c == tag && d == tag)) break;
    if ((spins & 63u) == 63u) {
      if (spins > SPIN_CAP) __hip_atomic_store((GAS u64*)(ws + WS_TMO), tag, RLX_AGENT);
      if (__hip_atomic_load((GAS u64*)(ws + WS_TMO), RLX_AGENT) == tag) break;
    }
  }
  __builtin_amdgcn_fence(__ATOMIC_ACQUIRE, "wavefront");
}

#define STAMP(i) do { __builtin_amdgcn_sched_barrier(0); ts[i] = (u32)__builtin_amdgcn_s_memrealtime(); asm volatile("s_waitcnt lgkmcnt(0)" : "+s"(ts[i])); __builtin_amdgcn_sched_barrier(0); } while (0)
#define TSOUT() do { if (tid == 0) { _Pragma("unroll") for (int i_ = 0; i_ < 24; ++i_) ((u32*)(ws + WS_DBG))[wg * 32 + i_] = ts[i_]; } } while (0)

template <int I, int N, typename F> DI void sfor(F&& f) {
  if constexpr (I < N) {
    f(std::integral_constant<int, I>{});
    sfor<I + 1, N>(f);
  }
}
#define SFOR(i, a, b) sfor<a, b>([&](auto i##_c) __attribute__((always_inline)) { constexpr int i = decltype(i##_c)::value;
#define SEND });

constexpr int NRES = 10, NSTREAM = 16 - NRES;
constexpr u32 sp_slot(int s) { return s < 2 ? L_SPA + s * 4096 : L_SPB + (s - 2) * 4096; }

__global__ void __launch_bounds__(256, 1)
fused_kernel(const void* a0, const void* a1, const void* a2, const void* a3, const void* a4, const void* a5, const void* a6, void* a7, char* a8) {
  u64 tA, tB;
  asm volatile("s_memrealtime %0" : "=s"(tA) :: "memory");
  typedef __attribute__((address_space(4))) const u64* kptr;
  kptr ka = (kptr)__builtin_amdgcn_kernarg_segment_ptr();
  asm volatile("" : "+s"(ka) :: "memory");
  const void* x_ = (const void*)ka[0]; const void* wqkv_ = (const void*)ka[1]; const void* wo_ = (const void*)ka[2]; const void* win_ = (const void*)ka[3];
  const void* wsp_ = (const void*)ka[4]; const void* bsp_ = (const void*)ka[5]; const void* wout_ = (const void*)ka[6]; void* out_ = (void*)ka[7]; char* ws = (char*)ka[8];
  asm volatile("s_waitcnt lgkmcnt(0)\n\ts_memrealtime %0\n\ts_waitcnt lgkmcnt(0)" : "=s"(tB), "+s"(tA), "+s"(x_), "+s"(ws) :: "memory");
  __shared__ __attribute__((aligned(1024))) char smem[LDS_BYTES];
  asm volatile("" ::"v"((u32)(uintptr_t)(LAS char*)smem));
  const u32 tid = threadIdx.x, l = tid & 63, w = rfl(tid >> 6), wg = blockIdx.x;
  const u32 xcd = wg & 7, rr_ = wg >> 3;
  const u32 p1 = 8 * (xcd >> 1) + (rr_ & 7), j = 4 * (xcd & 1) + (rr_ >> 3);
  const u32 id = p1 * 8 + j;
  const u32 P = p1 >> 1, g14 = 2 * j + (p1 & 1);
  u32 ts[24] = {};
  STAMP(0);
  const u32x4 sX = make_srd(x_, 2048 * 896), sWQKV = make_srd(wqkv_, 448 * 2688), sWS = make_srd(ws, WS_BYTES);
  const __amdgpu_buffer_rsrc_t rWS = __builtin_amdgcn_make_buffer_rsrc(ws, 0, WS_BYTES, 0x00020000);
  const u32 zero = 0;

  u32x2 epoch;
  ld64<0, true>(epoch, zero, sWS, WS_EPOCH);
  u32x4 xr[2][7];
  {
    const u32 vo = (64 * p1 + 16 * w + (l >> 3)) * 896 + (l & 7) * 16;
    SFOR(i, 0, 7) ld128<i * 128, false>(xr[0][i], vo, sX, zero); SEND
    SFOR(i, 0, 7) ld128<i * 128, false>(xr[1][i], vo, sX, 8 * 896); SEND
  }
  {
    u32 vkv[7];
    SFOR(m, 0, 7)
      const u32 c = 64 * w + l + 256 * m, row = c / 14, pos = c - 14 * row;
      vkv[m] = row * 2688 + 896 + 16 * pos + (pos >= 7 ? 784 : 0) + 112 * j;
    SEND
    SFOR(m, 0, 25) dma<0, false>(L_WKV + (w + 4 * m) * 1024, vkv[m % 7], sWQKV, (m / 7) * 128 * 2688); SEND
  }
  waitpin<39>(epoch);
  const u64 tag = (((u64)rfl(epoch.y) << 32) | rfl(epoch.x)) + 1;
  SFOR(r, 0, 2)
    waitvm<25 + (1 - r) * 7>();
    SFOR(i, 0, 7) pin(xr[r][i]); SEND
    f32x2 v[28];
    SFOR(i, 0, 7) SFOR(e, 0, 4) v[4 * i + e] = f32x2{bflo(xr[r][i][e]), bfhi(xr[r][i][e])}; SEND SEND
    f32x2 s2 = v[0];
    SFOR(i, 1, 28) s2 += v[i]; SEND
    float s = s2.x + s2.y;
    s = dpp_add<0xB1>(s); s = dpp_add<0x4E>(s); s = dpp_add<0x141>(s);
    const float mean = s * (1.0f / 448);
    f32x2 q2 = {0.f, 0.f};
    SFOR(i, 0, 28) v[i] -= f32x2{mean, mean}; q2 = __builtin_elementwise_fma(v[i], v[i], q2); SEND
    float q = q2.x + q2.y;
    q = dpp_add<0xB1>(q); q = dpp_add<0x4E>(q); q = dpp_add<0x141>(q);
    const float rs = __builtin_amdgcn_rsqf(q * (1.0f / 448) + 1e-5f);
    const u32 a = L_X + (16 * w + 8 * r + (l >> 3)) * XSTR + (l & 7) * 16;
    SFOR(i, 0, 7)
      u32x4 o;
      SFOR(e, 0, 4) const f32x2 t = v[4 * i + e] * f32x2{rs, rs}; o[e] = cvtpk(t.x, t.y); SEND
      *(LAS u32x4*)(a + i * 128) = o;
    SEND
  SEND
  STAMP(1);
  const u32 hh = w & 1, th = w >> 1, hf = l >> 5;
  const u32 aX = L_X + (32 * th + (l & 31)) * XSTR + hf * 16;
  const u32 trq = (l >> 2) & 3, trp = l & 3, blk = (l >> 4) & 1;
  {
    f32x16 ak = {}, av = {};
    const u32 aW = L_WKV + (8 * hf + trq) * 224 + 56 * hh + 32 * blk + 8 * trp;
    u32 qc = 64 * (w - 1) + l, qrow = qc / 7, qpos = qc - 7 * qrow;
    u32 vq = qrow * 2688 + 16 * qpos + 112 * j;
    SFOR(s, 0, 7)
      constexpr int cnt[7] = {4, 7, 11, 14, 18, 21, 25}, qb[8] = {0, 0, 4, 9, 14, 17, 17, 17};
      if (w == 0) waitvm<25 - cnt[s]>(); else waitvm<25 - cnt[s] + qb[s]>();
      bar();
      if (w != 0) {
        SFOR(m, qb[s], qb[s + 1])
          dma<0, false>(L_WQ + (w - 1 + 3 * m) * 1024, vq, sWQKV, zero);
          qpos += 3;
          const bool wrap = qpos >= 7;
          vq += wrap ? 28 * 2688 + 48 - 112 : 27 * 2688 + 48;
          qpos -= wrap ? 7 : 0;
        SEND
      }
      SFOR(kk, 0, 4)
        constexpr int ks = 4 * s + kk;
        const u32x4 xa = lds128(aX + ks * 32);
        const u32x4 bk = trfrag(aW + (16 * ks) * 224, aW + (16 * ks + 4) * 224);
        const u32x4 bv = trfrag(aW + (16 * ks) * 224 + 112, aW + (16 * ks + 4) * 224 + 112);
        ak = mfma32(xa, bk, ak);
        av = mfma32(xa, bv, av);
      SEND
    SEND
    STAMP(2);
    const u32 col = l & 31;
    u32x4 pk[2], pv[2];
    SFOR(i, 0, 8)
      const u32 a = cvtpk(phi(ak[2 * i]), phi(ak[2 * i + 1])), b = cvtpk(av[2 * i], av[2 * i + 1]);
      pk[i >> 2][i & 3] = col < 28 ? a : 0u;
      pv[i >> 2][i & 3] = col < 28 ? b : (col == 28 ? 0x3f803f80u : 0u);
    SEND
    f32x16 kv = {};
    kv = mfma32(pv[0], pk[0], kv);
    kv = mfma32(pv[1], pk[1], kv);
    const u32 a = L_KST + w * 4096 + hf * 512 + col * 4;
    SFOR(i, 0, 16) *(LAS float*)(a + ((i & 3) + 8 * (i >> 2)) * 128) = kv[i]; SEND
  }
  bar();
  if (w == 0) {
    const u32 quad = l & 7;
    SFOR(i, 0, 8)
      constexpr int hd = i >> 2;
      const u32 rho = 8 * (i & 3) + (l >> 3);
      const u32 e = rho < 16 ? rho : ((rho & 3) == 3 ? 28u : 16 + 3 * ((rho - 16) >> 2) + (rho & 3));
      const u32 a = L_KST + hd * 4096 + e * 128 + quad * 16;
      const f32x4 v = lds128f(a) + lds128f(a + 8192);
      st128(rWS, WS_KVP + ((j * 32 + (rho & ~1u) + hd) * 32 + p1) * 256 + (rho & 1) * 128 + quad * 16, __builtin_bit_cast(u32x4, v));
    SEND
    waitvm<0>();
    if (l == 0) set_flag(ws, F_KVP, id, tag);
  } else {
    waitvm<0>();
  }
  STAMP(3);
  bar();
  {
    f32x16 aq = {};
    const u32 aW = L_WQ + (8 * hf + trq) * 112 + 56 * hh + 32 * blk + 8 * trp;
    SFOR(ks, 0, 28)
      const u32x4 xa = lds128(aX + ks * 32);
      const u32x4 wq = trfrag(aW + (16 * ks) * 112, aW + (16 * ks + 4) * 112);
      aq = mfma32(wq, xa, aq);
    SEND
    u32x4 pq[2];
    SFOR(i, 0, 8)
      const u32 a = cvtpk(phi(aq[2 * i]), phi(aq[2 * i + 1]));
      pq[i >> 2][i & 3] = (i >= 6 && hf) ? 0u : a;
    SEND
    const u32 o = WS_PQ + ((p1 * 4 + 2 * th + blk) * 16 + 2 * j + hh) * 1024 + hf * 256 + (l & 15) * 16;
    st128(rWS, o, pq[0]);
    st128(rWS, o + 512, pq[1]);
  }
  waitvm<0>();
  bar();
  if (tid == 0) set_flag(ws, F_PQ, id, tag);
  STAMP(4);
  const u32x4 sWIN = make_srd(win_, 448 * 3584);
  const u32 vrow8 = l >> 3, vpos = l & 7;
  if (w != 0) {
    if (j < 7) {
      const u32x4 sWO = make_srd(wo_, 448 * 896);
      const u32 vo = vrow8 * 896 + (vpos ^ (((l >> 4) & 3) << 1)) * 16 + 128 * j;
      for (u32 n = w - 1; n < 56; n += 3) dma<0, false>(L_WO + n * 1024, vo, sWO, n * 8 * 896);
      const u32 vz = vrow8 * 3584 + (vpos ^ (((l >> 4) & 1) << 2)) * 16 + 128 * g14 + 1792;
      for (u32 n = w - 1; n < 56; n += 3) dma<0, false>(L_WZ2 + n * 1024, vz, sWIN, n * 8 * 3584);
    }
  } else {
    poll1(ws, flag_ptr(ws, F_KVP, (l & 31) * 8 + j), l < 32, tag);
    STAMP(5);
    u32x4 pr[8];
    const u32 vo = WS_KVP + (j * 32 + p1) * 8192 + l * 16;
    SFOR(i, 0, 4) ld128<i * 1024, true>(pr[i], vo, sWS, zero); SEND
    SFOR(i, 0, 4) ld128<i * 1024, true>(pr[4 + i], vo, sWS, 4096); SEND
    waitvm<0>();
    SFOR(i, 0, 8) pin(pr[i]); SEND
    f32x4 s = __builtin_bit_cast(f32x4, pr[0]);
    SFOR(i, 1, 8) s += __builtin_bit_cast(f32x4, pr[i]); SEND
    SFOR(e, 0, 4) s[e] = add_x32(add_x16(s[e])); SEND
    const u32 a = cvtpk(s[0], s[1]), b = cvtpk(s[2], s[3]);
    const u32 a2 = __builtin_amdgcn_update_dpp(0, (int)a, 0x4E, 0xf, 0xf, true), b2 = __builtin_amdgcn_update_dpp(0, (int)b, 0x4E, 0xf, 0xf, true);
    const u32 qd = l & 7, g = 2 * (qd >> 2) + (qd & 1), i = p1 >> 1, rq = (i & 7) >> 1;
    const u32 pos = 2 * (g ^ ((4 - rq) & 3)) + ((l >> 3) & 1);
    if (l < 16 && !(qd & 2)) st128(rWS, WS_KVR + ((2 * j + (p1 & 1)) * 16 + i) * 128 + pos * 16, u32x4{a, b, a2, b2});
    waitvm<0>();
    if (l == 0) set_flag(ws, F_KVR, id, tag);
    STAMP(6);
  }
  if (j == 7) {
    if (p1 < 28) {
      u32x4 wr[14];
      const u32 vo = (112 * w + vrow8) * 3584 + vpos * 16 + 128 * p1;
      SFOR(i, 0, 14) ld128<0, false>(wr[i], vo, sWIN, i * 8 * 3584); SEND
      waitvm<0>();
      f32x2 c[4] = {};
      SFOR(i, 0, 14) pin(wr[i]); SFOR(e, 0, 4) c[e] += f32x2{bflo(wr[i][e]), bfhi(wr[i][e])}; SEND SEND
      float cs[8];
      SFOR(e, 0, 8) cs[e] = add_x32(add_x16(dpp_add<0x128>(c[e >> 1][e & 1]))); SEND
      if (l < 8) {
        *(LAS f32x4*)(L_MISC + w * 256 + l * 32) = f32x4{cs[0], cs[1], cs[2], cs[3]};
        *(LAS f32x4*)(L_MISC + w * 256 + l * 32 + 16) = f32x4{cs[4], cs[5], cs[6], cs[7]};
      }
      bar();
      if (w == 0) {
        if (l < 16) {
          const u32 a = L_MISC + l * 16;
          const f32x4 t = (lds128f(a) + lds128f(a + 256)) + (lds128f(a + 512) + lds128f(a + 768));
          st128(rWS, WS_CS + p1 * 256 + l * 16, __builtin_bit_cast(u32x4, t));
        }
        waitvm<0>();
        if (l == 0) set_flag(ws, F_CS, p1, tag);
      }
    }
    TSOUT();
    return;
  }
  if (w == 0) {
    poll1(ws, flag_ptr(ws, F_PQ, p1 * 8 + (l & 7)), l < 8, tag);
    poll4(ws, flag_ptr(ws, F_KVR, l), tag);
    if (wg == 0 && l == 0) __hip_atomic_store((GAS u64*)(ws + WS_EPOCH), tag, RLX_AGENT);
  }
  bar();
  STAMP(7);
  const u32 t16 = l & 15, g4 = l >> 4;
  f32x4 hacc[4];
  {
    u32x4 kr[8], pq[16];
    u32x2 xt[4];
    const u32 vk = WS_KVR + w * 8192 + l * 16;
    SFOR(i, 0, 4) ld128<i * 1024, true>(kr[i], vk, sWS, zero); SEND
    SFOR(i, 0, 4) ld128<i * 1024, true>(kr[4 + i], vk, sWS, 4096); SEND
    const u32 vp = WS_PQ + (p1 * 4 + w) * 16384 + l * 16;
    SFOR(i, 0, 16) ld128<(i & 3) * 1024, true>(pq[i], vp, sWS, (i >> 2) * 4096); SEND
    const u32 vx = (64 * p1 + 16 * w + t16) * 896 + 128 * j + 8 * g4;
    SFOR(i, 0, 4) ld64<i * 32, false>(xt[i], vx, sX, zero); SEND
    waitvm<20>();
    SFOR(i, 0, 8) pin(kr[i]); *(LAS u32x4*)(L_KVR + w * 8192 + i * 1024 + l * 16) = kr[i]; SEND
    bar();
    STAMP(8);
    const u32 aK = L_KVR + (t16 >> 1) * 128 + (2 * (g4 ^ ((4 - (t16 >> 2)) & 3)) + (t16 & 1)) * 16;
    u32 aO[2][4];
    {
      const u32 r0 = 4 * g4 + trq, r1 = 16 + 3 * g4 + (trq < 2 ? trq : 2);
      SFOR(y, 0, 4)
        aO[0][y] = L_WO + r0 * 128 + ((y ^ ((r0 >> 1) & 3)) * 32) + 8 * trp;
        aO[1][y] = L_WO + r1 * 128 + ((y ^ ((r1 >> 1) & 3)) * 32) + 8 * trp;
      SEND
    }
    SFOR(n, 0, 4) hacc[n] = f32x4{0.f, 0.f, 0.f, 0.f}; SEND
    SFOR(h, 0, 16)
      waitpin<19 - h>(pq[h]);
      const f32x4 z4 = {0.f, 0.f, 0.f, 0.f};
      const f32x4 d0 = mfma16(lds128(aK + h * 2048), pq[h], z4), d1 = mfma16(lds128(aK + h * 2048 + 1024), pq[h], z4);
      const float r = __builtin_amdgcn_rcpf(d1[3] + 1e-6f);
      const u32x4 ob = {cvtpk(d0[0] * r, d0[1] * r), cvtpk(d0[2] * r, d0[3] * r), cvtpk(d1[0] * r, d1[1] * r), cvtpk(d1[2] * r, 0.f)};
      SFOR(n, 0, 4)
        constexpr int y = n ^ (2 * (h & 1));
        hacc[n] = mfma16(trfrag(aO[0][y] + h * 3584, aO[1][y] + h * 3584), ob, hacc[n]);
      SEND
    SEND
    waitvm<0>();
    float s = 0.f, q = 0.f;
    SFOR(n, 0, 4)
      pin(xt[n]);
      hacc[n] += f32x4{bflo(xt[n].x), bfhi(xt[n].x), bflo(xt[n].y), bfhi(xt[n].y)};
      SFOR(e, 0, 4) s += hacc[n][e]; q = __builtin_fmaf(hacc[n][e], hacc[n][e], q); SEND
    SEND
    const u32 o = WS_H + ((2 * p1 + (w >> 1)) * 28 + 4 * j + (g4 >> 1)) * 1024 + (g4 & 1) * 512 + (16 * (w & 1) + t16) * 16;
    SFOR(m, 0, 2)
      st128(rWS, o + m * 2048, u32x4{cvtpk(hacc[2 * m][0], hacc[2 * m][1]), cvtpk(hacc[2 * m][2], hacc[2 * m][3]), cvtpk(hacc[2 * m + 1][0], hacc[2 * m + 1][1]),
                                     cvtpk(hacc[2 * m + 1][2], hacc[2 * m + 1][3])});
    SEND
    s = add_x32(add_x16(s));
    q = add_x32(add_x16(q));
    if (l < 16) st64(rWS, WS_HST + ((P * 7 + j) * 128 + 64 * (p1 & 1) + 16 * w + l) * 8, u32x2{asu(s), asu(q)});
  }
  waitvm<0>();
  bar();
  if (tid == 0) set_flag(ws, F_H, id, tag);
  STAMP(9);
  if (w != 0) {
    const u32 vz = vrow8 * 3584 + (vpos ^ (((l >> 4) & 1) << 2)) * 16 + 128 * g14;
    for (u32 n = w - 1; n < 56; n += 3) dma<0, false>(L_WZ1 + n * 1024, vz, sWIN, n * 8 * 3584);
  } else {
    const u32 m = l < 14 ? l : 0, pp = 2 * P + (m >= 7), jj = m >= 7 ? m - 7 : m;
    GAS u64* fp = l < 14 ? flag_ptr(ws, F_H, pp * 8 + jj) : flag_ptr(ws, F_CS, g14 + (l & 1) * 14);
    poll1(ws, fp, l < 16, tag);
  }
  bar();
  STAMP(10);
  const u32 t32 = l & 31;
  const u32x4 sWSP = make_srd(wsp_, 2048 * 4096);
  u32 vS[2];
  SFOR(i, 0, 2) vS[i] = (128 * P + 32 * w + vrow8) * 4096 + (vpos ^ (4 * i + (l >> 4))) * 16; SEND
  const u32 spA = w * 8192, spB = w * 12288;
  auto sp_issue = [&](auto c_, auto s_) __attribute__((always_inline)) {
    constexpr int c = decltype(c_)::value, s = decltype(s_)::value;
    const u32 kb = ((P + (c >> 1)) & 15) * 256 + (c & 1) * 128;
    SFOR(i, 0, 4) dma<0, false>(sp_slot(s) + (s < 2 ? spA : spB) + i * 1024, vS[i & 1], sWSP, kb + i * 8 * 4096); SEND
  };
  u32 aS[2][4];
  SFOR(k, 0, 4)
    const u32 b = t32 * 128 + (((2 * k + hf) ^ ((t32 >> 1) & 7)) * 16);
    aS[0][k] = L_SPA + spA + b;
    aS[1][k] = L_SPB + spB + b;
  SEND
  u32x4 res[NRES * 8];
  auto sp_take = [&](auto c_, auto s_) __attribute__((always_inline)) {
    constexpr int c = decltype(c_)::value, s = decltype(s_)::value, o = (s < 2 ? s : s - 2) * 4096, r = (c - 2 * NSTREAM) * 4;
    take4<o, (r < 48)>(res[r], res[r + 1], res[r + 2], res[r + 3], aS[s >= 2][0], aS[s >= 2][1], aS[s >= 2][2], aS[s >= 2][3]);
  };
#define IC(v) std::integral_constant<int, (v)>{}
  u32 z1p[16];
  u32 bbits;
  {
    u32x4 hb[28];
    u32x2 st[4];
    u32x4 cs[8];
    const u32 vst = WS_HST + ((P * 7 + 4 * hf) * 128 + 32 * w + t32) * 8;
    SFOR(i, 0, 3) ld64<i * 1024, true>(st[i], vst, sWS, zero); SEND
    ld64<0, true>(st[3], vst + (hf ? 0 : 3072), sWS, zero);
    const u32 vh = WS_H + (4 * P + w) * 28 * 1024 + l * 16;
    SFOR(i, 0, 28) ld128<(i & 3) * 1024, true>(hb[i], vh, sWS, (i >> 2) * 4096); SEND
    const u32 vc2 = WS_CS + (896 + 64 * g14 + 4 * hf) * 4;
    SFOR(i, 0, 8) ld128<i * 32, true>(cs[i], vc2, sWS, zero); SEND
    {
      const u32x4 sB = make_srd(bsp_, 4096);
      asm volatile("buffer_load_ushort %0, %1, %2, 0 offen" : "=v"(bbits) : "v"((128 * P + 32 * w + t32) * 2), "s"(sB) : "memory");
    }
    const u32 rowp = (4 * hf + trq) * 128 + blk * 32;
    const u32 aZ2[2] = {L_WZ2 + rowp + ((trq >> 1) * 64) + 8 * trp, L_WZ2 + rowp + (((trq >> 1) ^ 1) * 64) + 8 * trp};
    const u32 pp = 8 * (2 * (trp & 1) + (trp >> 1));
    const u32 aZ1[2] = {L_WZ1 + rowp + ((trq >> 1) * 64) + pp, L_WZ1 + rowp + (((trq >> 1) ^ 1) * 64) + pp};
    f32x16 z[2] = {};
    SFOR(k, 0, 28)
      if constexpr ((k & 1) == 0) { waitpin<9 + 26 - k>(hb[k]); pin(hb[k + 1]); }
      constexpr int ro = (32 * (k >> 1) + 8 * (k & 1)) * 128;
      SFOR(f, 0, 2) z[f] = mfma32(trfrag(aZ2[f] + ro, aZ2[f] + ro + 2048), hb[k], z[f]); SEND
    SEND
    waitvm<0>();
    STAMP(11);
    SFOR(i, 0, 4) pin(st[i]); SEND
    SFOR(i, 0, 8) pin(cs[i]); SEND
    pin(bbits);
    float rs, nm;
    {
      float s = asf(st[0].x) + asf(st[1].x) + asf(st[2].x);
      float q = asf(st[0].y) + asf(st[1].y) + asf(st[2].y);
      s += hf ? 0.f : asf(st[3].x);
      q += hf ? 0.f : asf(st[3].y);
      s = add_x32(s) * (1.0f / 448);
      q = add_x32(q) * (1.0f / 448);
      rs = __builtin_amdgcn_rsqf(__builtin_fmaxf(q - s * s, 0.f) + 1e-5f);
      nm = -s * rs;
    }
    f32x2 zz[16];
    {
      f32x2 s2 = {0.f, 0.f}, q2 = {0.f, 0.f};
      SFOR(i, 0, 16)
        constexpr int f = i >> 3, e = (2 * i) & 15;
        const f32x4 c = __builtin_bit_cast(f32x4, cs[i >> 1]);
        const f32x2 u = __builtin_elementwise_fma(f32x2{c[(2 * i) & 3], c[((2 * i) & 3) + 1]}, f32x2{nm, nm}, f32x2{z[f][e], z[f][e + 1]} * f32x2{rs, rs});
        zz[i] = gelu2(u);
        s2 += zz[i];
        q2 = __builtin_elementwise_fma(zz[i], zz[i], q2);
      SEND
      const float s = add_x32(s2.x + s2.y), q = add_x32(q2.x + q2.y);
      if (l < 32) st64(rWS, WS_ZST + ((P * 14 + g14) * 128 + 32 * w + l) * 8, u32x2{asu(s), asu(q)});
    }
    waitvm<0>();
    bar();
    if (tid == 0) set_flag(ws, F_ZS, id, tag);
    STAMP(12);
    const u32 vc1 = WS_CS + (64 * g14 + 8 * hf) * 4;
    SFOR(i, 0, 8) ld128<(i >> 2) * 128 + ((i >> 1) & 1) * 64 + (i & 1) * 16, true>(cs[i], vc1, sWS, zero); SEND
    SFOR(s, 0, 5) sp_issue(IC(2 * NSTREAM + s), IC(s)); SEND
    z[0] = f32x16{};
    z[1] = f32x16{};
    SFOR(k, 0, 28)
      constexpr int ro = (32 * (k >> 1) + 8 * (k & 1)) * 128;
      SFOR(f, 0, 2) z[f] = mfma32(trfrag(aZ1[f] + ro, aZ1[f] + ro + 2048), hb[k], z[f]); SEND
      if constexpr (k >= 8 && (k & 3) == 0) {
        constexpr int s = (k - 8) >> 2;
        waitvm<16>();
        sp_take(IC(2 * NSTREAM + s), IC(s));
        sp_issue(IC(2 * NSTREAM + 5 + s), IC(s));
      }
    SEND
    STAMP(13);
    if (w == 0) {
      const u32 m = l < 14 ? l : 0, pp2 = 2 * P + (m >= 7), jj = m >= 7 ? m - 7 : m;
      poll1(ws, flag_ptr(ws, F_ZS, pp2 * 8 + jj), l < 14, tag);
    }
    bar();
    STAMP(14);
    {
      u32x2 zs[7];
      const u32 vz = WS_ZST + ((P * 14 + 7 * hf) * 128 + 32 * w + t32) * 8;
      SFOR(i, 0, 7) ld64<(i & 3) * 1024, true>(zs[i], vz, sWS, (i >> 2) * 4096); SEND
      waitvm<0>();
      float s = 0.f, q = 0.f;
      SFOR(i, 0, 7) pin(zs[i]); s += asf(zs[i].x); q += asf(zs[i].y); SEND
      s = add_x32(s) * (1.0f / 896);
      q = add_x32(q) * (1.0f / 896);
      const float r2 = __builtin_amdgcn_rsqf(__builtin_fmaxf(q - s * s, 0.f) + 1e-5f), n2 = -s * r2;
      const u32 o = WS_Z + (g14 * 64 + 4 * P + w) * 4096 + hf * 512;
      SFOR(c, 0, 4)
        u32x4 v;
        SFOR(e, 0, 4) const f32x2 t = __builtin_elementwise_fma(zz[4 * c + e], f32x2{r2, r2}, f32x2{n2, n2}); v[e] = cvtpk(t.x, t.y); SEND
        st128(rWS, o + c * 1024 + (t32 ^ ((2 * (c & 1) + hf) << 2)) * 16, v);
      SEND
    }
    waitvm<0>();
    bar();
    if (tid == 0) set_flag(ws, F_Z, id, tag);
    STAMP(15);
    SFOR(s, 0, 5) sp_take(IC(2 * NSTREAM + 5 + s), IC(s)); if constexpr (10 + s < 2 * NRES) sp_issue(IC(2 * NSTREAM + 10 + s), IC(s)); SEND
    SFOR(i, 0, 8) pin(cs[i]); SEND
    SFOR(i, 0, 16)
      constexpr int f = i >> 3, e = (2 * i) & 15;
      const f32x4 c = __builtin_bit_cast(f32x4, cs[i >> 1]);
      const f32x2 u = __builtin_elementwise_fma(f32x2{c[(2 * i) & 3], c[((2 * i) & 3) + 1]}, f32x2{nm, nm}, f32x2{z[f][e], z[f][e + 1]} * f32x2{rs, rs});
      const f32x2 t = gelu2(u);
      z1p[i] = cvtpk(t.x, t.y);
    SEND
  }
  static_assert(NRES == 10, "the schedule below is written for 20 resident chunks");
  SFOR(s, 0, 5) waitvm<16>(); sp_take(IC(2 * NSTREAM + 10 + s), IC(s)); sp_issue(IC(2 * NSTREAM + 15 + s), IC(s)); SEND
  SFOR(s, 0, 5) waitvm<16>(); sp_take(IC(2 * NSTREAM + 15 + s), IC(s)); sp_issue(IC(s), IC(s)); SEND
  STAMP(16);
  if (w == 0) poll1(ws, flag_ptr(ws, F_Z, (2 * (l & 15) + (p1 & 1)) * 8 + j), l < 16, tag);
  bar();
  STAMP(17);
  {
    const u32 vZ = WS_Z + g14 * 64 * 4096 + w * 4096 + l * 16;
    auto z_issue = [&](auto st_) __attribute__((always_inline)) {
      constexpr int st = decltype(st_)::value;
      const u32 so = ((P + st) & 15) * 16384, la = L_ZR + (st & 3) * 16384 + w * 4096;
      SFOR(i, 0, 4) dma<i * 1024, true>(la, vZ, sWS, so); SEND
    };
    SFOR(st, 0, 3) z_issue(IC(st)); SEND
    u32 aZ[4];
    {
      const u32 o3 = 2 * blk + (trp >> 1), b0 = L_ZR + o3 * 512 + (((2 * hf) ^ o3) * 4 + trq) * 16 + (trp & 1) * 8;
      SFOR(v, 0, 4) aZ[v] = b0 ^ (64 * (v & 1)) ^ (256 * (v >> 1)); SEND
    }
    f32x16 acc[2] = {};
    SFOR(st, 0, 16)
      constexpr int spc = (st >= 1 && 2 * (st - 1) + 5 < 2 * NSTREAM ? 1 : 0) + (st >= 1 && 2 * (st - 1) + 6 < 2 * NSTREAM ? 1 : 0)
                        + (st >= 2 && 2 * (st - 2) + 5 < 2 * NSTREAM ? 1 : 0) + (st >= 2 && 2 * (st - 2) + 6 < 2 * NSTREAM ? 1 : 0)
                        + (st >= 3 && 2 * (st - 3) + 5 < 2 * NSTREAM ? 1 : 0) + (st >= 3 && 2 * (st - 3) + 6 < 2 * NSTREAM ? 1 : 0);
      constexpr int zl = (st + 1 < 16 ? 1 : 0) + (st + 2 < 16 ? 1 : 0);
      waitvm<4 * (zl + spc)>();
      bar();
      if constexpr (st + 3 < 16) z_issue(IC(st + 3));
      SFOR(cc, 0, 2)
        constexpr int c = 2 * st + cc;
        u32x4 bs[4];
        if constexpr (st < NSTREAM) {
          constexpr int s = c % 5;
          if constexpr (c >= 5) {
            constexpr int ist = (c - 5) >> 1;
            constexpr int zy = (st - ist) - ((st + 3 < 16) ? 0 : 0);
            constexpr int cy = (c + 4 < 2 * NSTREAM ? 4 : 2 * NSTREAM - 1 - c);
            waitvm<4 * (zy + cy)>();
          }
          SFOR(k, 0, 4) bs[k] = lds128(aS[s >= 2][k] + (s < 2 ? s : s - 2) * 4096); SEND
          if constexpr (c + 5 < 2 * NSTREAM) {
            asm volatile("s_waitcnt lgkmcnt(0)" : "+v"(bs[0]), "+v"(bs[1]), "+v"(bs[2]), "+v"(bs[3])::"memory");
            sp_issue(IC(c + 5), IC(s));
          }
        }
        SFOR(k, 0, 4)
          constexpr int ks = 4 * cc + k, off = (st & 3) * 16384 + (ks >> 1) * 4096;
          SFOR(f, 0, 2)
            const u32x4 a = trfrag(aZ[2 * (ks & 1)] + off + f * 2048, aZ[2 * (ks & 1) + 1] + off + f * 2048);
            if constexpr (st < NSTREAM) acc[f] = mfma32(a, bs[k], acc[f]);
            else acc[f] = mfma32(a, res[(c - 2 * NSTREAM) * 4 + k], acc[f]);
          SEND
        SEND
      SEND
    SEND
    STAMP(18);
    const float b = bflo(bbits);
    const u32 o = WS_Y + ((8 * P + 2 * w + blk) * 28 + 2 * g14) * 1024 + hf * 256 + (l & 15) * 16;
    SFOR(c, 0, 4)
      u32x4 v;
      SFOR(e, 0, 4)
        constexpr int i = 8 * (c & 1) + 2 * e;
        const u32 zp = z1p[4 * c + e];
        v[e] = cvtpk(bflo(zp) * (acc[c >> 1][i] + b), bfhi(zp) * (acc[c >> 1][i + 1] + b));
      SEND
      st128(rWS, o + (c >> 1) * 1024 + (c & 1) * 512, v);
    SEND
  }
  waitvm<0>();
  bar();
  if (tid == 0) set_flag(ws, F_Y, id, tag);
  STAMP(19);
  if (w != 0) {
    const u32x4 sWOUT = make_srd(wout_, 896 * 896);
    const u32 x0 = (l >> 4) & 1;
    const u32 vo0 = vrow8 * 896 + (vpos ^ (x0 << 1)) * 16 + 128 * j, vo1 = vrow8 * 896 + (vpos ^ ((x0 | 2) << 1)) * 16 + 128 * j;
    for (u32 n = w - 1; n < 112; n += 3) dma<0, false>(L_WOUT + n * 1024, (n & 1) ? vo1 : vo0, sWOUT, n * 8 * 896);
  } else {
    const u32 m = l < 14 ? l : 0, pp = 2 * P + (m >= 7), jj = m >= 7 ? m - 7 : m;
    poll1(ws, flag_ptr(ws, F_Y, pp * 8 + jj), l < 14, tag);
  }
  bar();
  STAMP(20);
  {
    u32x4 yb[28];
    const u32 vy = WS_Y + (4 * p1 + w) * 28 * 1024 + l * 16;
    SFOR(i, 0, 28) ld128<(i & 3) * 1024, true>(yb[i], vy, sWS, (i >> 2) * 4096); SEND
    waitvm<28>();
    bar();
    STAMP(21);
    u32 aW[4];
    {
      const u32 xs = (trq >> 1) | ((g4 & 1) << 1);
      SFOR(n, 0, 4) aW[n] = L_WOUT + (8 * g4 + trq) * 128 + ((n ^ xs) * 32) + 8 * trp; SEND
    }
    SFOR(k, 0, 28)
      if constexpr ((k & 1) == 0) { waitpin<26 - k>(yb[k]); pin(yb[k + 1]); }
      SFOR(n, 0, 4) hacc[n] = mfma16(trfrag(aW[n] + k * 4096, aW[n] + k * 4096 + 512), yb[k], hacc[n]); SEND
    SEND
    STAMP(22);
    const __amdgpu_buffer_rsrc_t rO = __builtin_amdgcn_make_buffer_rsrc(out_, 0, 2048 * 896, 0x00020000);
    const u32 o = (64 * p1 + 16 * w + t16) * 896 + 128 * j + (g4 & 1) * 32 + (g4 >> 1) * 16;
    SFOR(m, 0, 2)
      const auto r0 = __builtin_amdgcn_permlane16_swap(cvtpk(hacc[2 * m][0], hacc[2 * m][1]), cvtpk(hacc[2 * m + 1][0], hacc[2 * m + 1][1]), false, false);
      const auto r1 = __builtin_amdgcn_permlane16_swap(cvtpk(hacc[2 * m][2], hacc[2 * m][3]), cvtpk(hacc[2 * m + 1][2], hacc[2 * m + 1][3]), false, false);
      __builtin_amdgcn_raw_buffer_store_b128(u32x4{(u32)r0[0], (u32)r1[0], (u32)r0[1], (u32)r1[1]}, rO, o + m * 64, 0, 0);
    SEND
  }
  STAMP(23);
  TSOUT();
  if (wg == 0) { const u32 until = ts[23] + AMP * ((u32)tB - (u32)tA); while ((int)((u32)__builtin_amdgcn_s_memrealtime() - until) < 0) __builtin_amdgcn_s_sleep(2); }
}

}

extern "C" void kernel_launch(void* const* d_in, const int* in_sizes, int n_in, void* d_out, int out_size, void* d_ws, size_t ws_size, hipStream_t stream) {
  fused_kernel<<<256, 256, 0, stream>>>(d_in[0], d_in[1], d_in[2], d_in[4], d_in[5], d_in[6], d_in[7], d_out, (char*)d_ws);
}
```

```cpp
#include <hip/hip_runtime.h>
#include <hip/hip_bf16.h>
#include <stdint.h>
#include <type_traits>
#pragma clang diagnostic ignored "-Wint-to-pointer-cast"

typedef unsigned u32;
typedef unsigned long long u64;
typedef u32 u32x2 __attribute__((ext_vector_type(2)));
typedef u32 u32x4 __attribute__((ext_vector_type(4)));
__device__ void raw_st128(u32x4 v, u32x4 srd, u32 voff, u32 soff, int aux) __asm("llvm.amdgcn.raw.buffer.store.v4i32");
__device__ void raw_st64(u32x2 v, u32x4 srd, u32 voff, u32 soff, int aux) __asm("llvm.amdgcn.raw.buffer.store.v2i32");

namespace {

#define DI __device__ __forceinline__
#define LAS __attribute__((address_space(3)))
#define GAS __attribute__((address_space(1)))
typedef float f32x2 __attribute__((ext_vector_type(2)));
typedef float f32x4 __attribute__((ext_vector_type(4)));
typedef float f32x16 __attribute__((ext_vector_type(16)));
typedef __bf16 bf16x8 __attribute__((ext_vector_type(8)));
typedef __bf16 bf16x2 __attribute__((ext_vector_type(2)));
typedef short s16x4 __attribute__((ext_vector_type(4)));
#define RLX_AGENT __ATOMIC_RELAXED, __HIP_MEMORY_SCOPE_AGENT

constexpr u32 WS_EPOCH = 0, WS_TMO = 128;
constexpr u32 WS_INIT = 4096;
constexpr u32 WS_FLH = 512u << 10, WS_FLZ = 640u << 10;
constexpr u32 WS_FLG = 768u << 10, WS_FLY = WS_FLG + 4096;
constexpr u32 WS_KVP = 1u << 20;
constexpr u32 WS_KVR = WS_KVP + (2u << 20);
constexpr u32 WS_CS = WS_KVR + 8 * 32768;
constexpr u32 WS_HST = WS_CS + 8192;
constexpr u32 WS_ZST = WS_HST + 16 * 7 * 128 * 8;
constexpr u32 WS_PQ = 4u << 20;
constexpr u32 WS_H = 6u << 20;
constexpr u32 WS_Z = 8u << 20;
constexpr u32 WS_Y = 12u << 20;
constexpr u32 WS_G = 16u << 20;
constexpr u32 WS_BYTES = 21u << 20;
static_assert(WS_ZST + 16 * 14 * 128 * 8 <= WS_PQ, "ws map");

constexpr u32 L_X = 0, XSTR = 912;
constexpr u32 L_WKV = 64 * XSTR;
constexpr u32 L_WQ = L_WKV;
constexpr u32 L_KST = L_WKV + 57344, KSTR = 144;
constexpr u32 L_XT = L_KST + 128 * KSTR;
constexpr u32 L_WO = 0;
constexpr u32 L_RED = 114688;
constexpr u32 L_WZ2 = 57344;
constexpr u32 L_WZ1 = 0;
constexpr u32 L_ZR = 0;
constexpr u32 L_SPA = 114688, L_SPB = 65536;
constexpr u32 L_WOUT = 65536, L_WOUT2 = 0;
constexpr u32 L_TRA = 57344, L_TRB = 147456;
constexpr u32 L_MISC = 160768;
constexpr u32 LDS_BYTES = 163840;

constexpr u32 SPIN_CAP = 1u << 16;
constexpr float LOG2E = 1.4426950408889634f;

DI u32 rfl(u32 x) { return __builtin_amdgcn_readfirstlane(x); }
DI u32x4 make_srd(const void* p, u32 bytes) {
  const u64 a = (u64)p;
  return u32x4{(u32)a, (u32)(a >> 32) & 0xffffu, bytes, 0x00020000u};
}
DI u32 cvtpk(float lo, float hi) {
  f32x2 v = {lo, hi};
  return __builtin_bit_cast(u32, __builtin_convertvector(v, bf16x2));
}
DI float asf(u32 x) { return __builtin_bit_cast(float, x); }
DI u32 asu(float x) { return __builtin_bit_cast(u32, x); }
DI float bflo(u32 x) { return __builtin_bit_cast(float, x << 16); }
DI float bfhi(u32 x) { return __builtin_bit_cast(float, x & 0xffff0000u); }
DI void mm32(f32x16& c, const u32x4& a, const u32x4& b) { asm volatile("v_mfma_f32_32x32x16_bf16 %0, %1, %2, %0" : "+v"(c) : "v"(a), "v"(b)); }
template <int R> DI void mm32a(f32x16& c, const u32x4& a) { asm volatile("v_mfma_f32_32x32x16_bf16 %0, %1, a[%c2:%c3], %0" : "+v"(c) : "v"(a), "i"(R), "i"(R + 3)); }
DI void mm32z(f32x16& c, const u32x4& a, const u32x4& b) { asm volatile("v_mfma_f32_32x32x16_bf16 %0, %1, %2, 0" : "=&v"(c) : "v"(a), "v"(b)); }
template <int R> DI void mm32A(f32x16& c, const u32x4& b) { asm volatile("v_mfma_f32_32x32x16_bf16 %0, a[%c2:%c3], %1, %0" : "+v"(c) : "v"(b), "i"(R), "i"(R + 3)); }
template <int R> DI void mm32Az(f32x16& c, const u32x4& b) { asm volatile("v_mfma_f32_32x32x16_bf16 %0, a[%c2:%c3], %1, 0" : "=&v"(c) : "v"(b), "i"(R), "i"(R + 3)); }
template <int R> DI void mm16az(f32x4& c, const u32x4& b) { asm volatile("v_mfma_f32_16x16x32_bf16 %0, a[%c2:%c3], %1, 0" : "=&v"(c) : "v"(b), "i"(R), "i"(R + 3)); }
DI void mm16c2(f32x4& c0, f32x4& c1, const u32x4& a0, const u32x4& a1, const u32x4& b, const f32x4& i1) {
  asm volatile("v_mfma_f32_16x16x32_bf16 %0, %2, %4, 0\n\tv_mfma_f32_16x16x32_bf16 %1, %3, %4, %5" : "=&v"(c0), "=&v"(c1) : "v"(a0), "v"(a1), "v"(b), "v"(i1));
}
DI void mm16(f32x4& c, const u32x4& a, const u32x4& b) { asm volatile("v_mfma_f32_16x16x32_bf16 %0, %1, %2, %0" : "+v"(c) : "v"(a), "v"(b)); }
template <int R> DI void mm16a(f32x4& c, const u32x4& b) { asm volatile("v_mfma_f32_16x16x32_bf16 %0, a[%c2:%c3], %1, %0" : "+v"(c) : "v"(b), "i"(R), "i"(R + 3)); }
DI void mm16z(f32x4& c, const u32x4& a, const u32x4& b) { asm volatile("v_mfma_f32_16x16x32_bf16 %0, %1, %2, 0" : "=v"(c) : "v"(a), "v"(b)); }
DI void mm16z2(f32x4& c0, f32x4& c1, const u32x4& a0, const u32x4& a1, const u32x4& b) {
  asm volatile("v_mfma_f32_16x16x32_bf16 %0, %2, %4, 0\n\tv_mfma_f32_16x16x32_bf16 %1, %3, %4, 0" : "=&v"(c0), "=v"(c1) : "v"(a0), "v"(a1), "v"(b));
}
DI void mm16x2(f32x4& c0, f32x4& c1, const u32x4& a0, const u32x4& a1, const u32x4& b) {
  asm volatile("v_mfma_f32_16x16x32_bf16 %0, %2, %4, %0\n\tv_mfma_f32_16x16x32_bf16 %1, %3, %4, %1" : "+v"(c0), "+v"(c1) : "v"(a0), "v"(a1), "v"(b));
}
template <int R> DI void mm32az(f32x16& c, const u32x4& a) { asm volatile("v_mfma_f32_32x32x16_bf16 %0, %1, a[%c2:%c3], 0" : "=&v"(c) : "v"(a), "i"(R), "i"(R + 3)); }
template <int N, typename A> DI void nops(A& a) { asm volatile("s_nop %1" : "+v"(a) : "i"(N - 1)); }
template <int N, typename A, typename B> DI void nops(A& a, B& b) { asm volatile("s_nop %2" : "+v"(a), "+v"(b) : "i"(N - 1)); }
template <int N, typename A> DI void nops4(A (&a)[4]) { asm volatile("s_nop %4" : "+v"(a[0]), "+v"(a[1]), "+v"(a[2]), "+v"(a[3]) : "i"(N - 1)); }
DI float dot2(u32 a, u32 b, float c) { return __builtin_amdgcn_fdot2_f32_bf16(__builtin_bit_cast(bf16x2, a), __builtin_bit_cast(bf16x2, b), c, false); }
DI u32 umax3(u32 a, u32 b, u32 c) { return max(max(a, b), c); }
DI bool isnanr(float x) { return x != x; }
DI bool isnan_(float x) { return x != x; }
DI u64 lanes_eq(u32 a, u32 b) { return __builtin_amdgcn_uicmp(a, b, 32); }
DI u64 lanes_ne(u32x2 a, u64 b) { return __builtin_amdgcn_uicmpl(__builtin_bit_cast(u64, a), b, 33); }
DI u32x2 tr64(u32 a) { return __builtin_bit_cast(u32x2, __builtin_amdgcn_ds_read_tr16_b64_v4i16((LAS s16x4*)a)); }
DI u32x4 trfrag(u32 a0, u32 a1) {
  const u32x2 lo = tr64(a0), hi = tr64(a1);
  return u32x4{lo.x, lo.y, hi.x, hi.y};
}
DI u32x4 lds128(u32 a) { return *(LAS u32x4*)a; }
DI f32x4 lds128f(u32 a) { return *(LAS f32x4*)a; }
DI void bar() { asm volatile("s_waitcnt lgkmcnt(0)\n\ts_barrier" ::: "memory"); }
template <int N> DI void waitvm() { asm volatile("s_waitcnt vmcnt(%0)" ::"i"(N) : "memory"); }
DI void setm0(u32 a) { asm volatile("s_mov_b32 m0, %0\n\ts_nop 0" ::"s"(a) : "memory"); }
template <u32 C, bool NOP = true> DI void setm0(u32 a) {
  if constexpr (NOP) asm volatile("s_add_u32 m0, %0, %1\n\ts_nop 0" ::"s"(a), "i"(C) : "memory", "scc");
  else asm volatile("s_add_u32 m0, %0, %1" ::"s"(a), "i"(C) : "memory", "scc");
}
#define RARE(x) __builtin_expect((x), 0)
template <typename T> DI void spin(T& a) { asm volatile("" : "+s"(a)); }
template <int IMM, bool SC1 = false> DI void dman(u32 voff, u32x4 srd, u32 soff) {
  if constexpr (SC1) asm volatile("buffer_load_dwordx4 %0, %1, %2 offen offset:%3 sc1 lds" ::"v"(voff), "s"(srd), "s"(soff), "i"(IMM) : "memory");
  else asm volatile("buffer_load_dwordx4 %0, %1, %2 offen offset:%3 lds" ::"v"(voff), "s"(srd), "s"(soff), "i"(IMM) : "memory");
}
DI void touch(u32 voff, u32x4 srd, u32 soff) {
  asm volatile("s_mov_b32 m0, %3\n\ts_nop 0\n\tbuffer_load_dword %0, %1, %2 offen lds" ::"v"(voff), "s"(srd), "s"(soff), "s"(L_MISC + 2048) : "memory");
}
template <int IMM, bool SC1> DI void ld128(u32x4& d, u32 voff, u32x4 srd, u32 soff) {
  if constexpr (SC1) asm volatile("buffer_load_dwordx4 %0, %1, %2, %3 offen offset:%4 sc1" : "=v"(d) : "v"(voff), "s"(srd), "s"(soff), "i"(IMM) : "memory");
  else asm volatile("buffer_load_dwordx4 %0, %1, %2, %3 offen offset:%4" : "=v"(d) : "v"(voff), "s"(srd), "s"(soff), "i"(IMM) : "memory");
}
template <bool SC1> DI void ld32(u32& d, u32 voff, u32x4 srd, u32 soff) {
  if constexpr (SC1) asm volatile("buffer_load_dword %0, %1, %2, %3 offen sc1" : "=v"(d) : "v"(voff), "s"(srd), "s"(soff) : "memory");
  else asm volatile("buffer_load_dword %0, %1, %2, %3 offen" : "=v"(d) : "v"(voff), "s"(srd), "s"(soff) : "memory");
}
DI void ld16(u32& d, u32 voff, u32x4 srd) { asm volatile("buffer_load_ushort %0, %1, %2, 0 offen" : "=v"(d) : "v"(voff), "s"(srd) : "memory"); }
template <int IMM, bool SC1> DI void ld64(u32x2& d, u32 voff, u32x4 srd, u32 soff) {
  if constexpr (SC1) asm volatile("buffer_load_dwordx2 %0, %1, %2, %3 offen offset:%4 sc1" : "=v"(d) : "v"(voff), "s"(srd), "s"(soff), "i"(IMM) : "memory");
  else asm volatile("buffer_load_dwordx2 %0, %1, %2, %3 offen offset:%4" : "=v"(d) : "v"(voff), "s"(srd), "s"(soff), "i"(IMM) : "memory");
}
template <int N, typename T> DI void waitpin(T& a) { asm volatile("s_waitcnt vmcnt(%1)" : "+v"(a) : "i"(N) : "memory"); }
template <typename T> DI void pin(T& a) { asm volatile("" : "+v"(a)); }
template <int R, int OFF> DI void take4(const u32 (&a)[4]) {
  asm volatile("ds_read_b128 a[%c5:%c6], %0 offset:%4\n\tds_read_b128 a[%c7:%c8], %1 offset:%4\n\tds_read_b128 a[%c9:%c10], %2 offset:%4\n\tds_read_b128 a[%c11:%c12], %3 offset:%4"
               ::"v"(a[0]), "v"(a[1]), "v"(a[2]), "v"(a[3]), "i"(OFF), "i"(R), "i"(R + 3), "i"(R + 4), "i"(R + 7), "i"(R + 8), "i"(R + 11), "i"(R + 12), "i"(R + 15) : "memory");
}
template <int R, int OFF> DI void take1(u32 a) { asm volatile("ds_read_b128 a[%c2:%c3], %0 offset:%1" ::"v"(a), "i"(OFF), "i"(R), "i"(R + 3) : "memory"); }
template <int R, int OFF0, int OFF1> DI void taketr(u32 a0, u32 a1) {
  asm volatile("ds_read_b64_tr_b16 a[%c4:%c5], %0 offset:%2\n\tds_read_b64_tr_b16 a[%c6:%c7], %1 offset:%3"
               ::"v"(a0), "v"(a1), "i"(OFF0), "i"(OFF1), "i"(R), "i"(R + 1), "i"(R + 2), "i"(R + 3) : "memory");
}
DI void own_acc_file() { asm volatile("" ::: "a0", "a255"); }
DI void waitlds() { asm volatile("s_waitcnt lgkmcnt(0)" ::: "memory"); }
DI void st128(u32x4 srd, u32 off, u32x4 v) { raw_st128(v, srd, off, 0, 16); }
DI void st64(u32x4 srd, u32 off, u32x2 v) { raw_st64(v, srd, off, 0, 16); }
template <int CTRL> DI float dpp_add(float x) {
  return x + __builtin_bit_cast(float, __builtin_amdgcn_update_dpp(0, __builtin_bit_cast(int, x), CTRL, 0xf, 0xf, true));
}
DI float add_x16(float x) {
  const auto r = __builtin_amdgcn_permlane16_swap(__builtin_bit_cast(u32, x), __builtin_bit_cast(u32, x), false, false);
  return __builtin_bit_cast(float, (u32)r[0]) + __builtin_bit_cast(float, (u32)r[1]);
}
DI float add_x32(float x) {
  const auto r = __builtin_amdgcn_permlane32_swap(__builtin_bit_cast(u32, x), __builtin_bit_cast(u32, x), false, false);
  return __builtin_bit_cast(float, (u32)r[0]) + __builtin_bit_cast(float, (u32)r[1]);
}
DI float phi(float t) { return __builtin_fmaxf(t, 1e-6f) + __builtin_amdgcn_fmed3f(__builtin_amdgcn_exp2f(t * LOG2E), 0.f, 1.f); }
DI f32x2 gelu2(f32x2 v, float k1, float k3) {
  const f32x2 p = __builtin_elementwise_fma(v * v, f32x2{k3, k3}, f32x2{k1, k1}) * v;
  const f32x2 e = f32x2{__builtin_amdgcn_exp2f(p.x), __builtin_amdgcn_exp2f(p.y)} + f32x2{1.f, 1.f};
  return v * f32x2{__builtin_amdgcn_rcpf(e.x), __builtin_amdgcn_rcpf(e.y)};
}
DI f32x2 gelu2s(f32x2 v, float k1, float k3, float ri) {
  const f32x2 p = __builtin_elementwise_fma(v * v, f32x2{k3, k3}, f32x2{k1, k1}) * v;
  const f32x2 e = __builtin_elementwise_fma(f32x2{__builtin_amdgcn_exp2f(p.x), __builtin_amdgcn_exp2f(p.y)}, f32x2{ri, ri}, f32x2{ri, ri});
  return v * f32x2{__builtin_amdgcn_rcpf(e.x), __builtin_amdgcn_rcpf(e.y)};
}
constexpr u32 SENT = 0xffffffffu;
DI bool giveup(u32& spins, char* ws, u64 tag) {
  if ((++spins & 63u) != 0) return false;
  if (spins > SPIN_CAP) __hip_atomic_store((GAS u64*)(ws + WS_TMO), tag, RLX_AGENT);
  return __hip_atomic_load((GAS u64*)(ws + WS_TMO), RLX_AGENT) == tag;
}


template <int I, int N, typename F> DI void sfor(F&& f) {
  if constexpr (I < N) {
    f(std::integral_constant<int, I>{});
    sfor<I + 1, N>(f);
  }
}
#define SFOR(i, a, b) sfor<a, b>([&](auto i##_c) __attribute__((always_inline)) { constexpr int i = decltype(i##_c)::value;
#define SEND });

#define SB() __builtin_amdgcn_sched_barrier(0)
#define IC(v) std::integral_constant<int, (v)>{}
#define LAM(...) [&](__VA_ARGS__) __attribute__((always_inline))
#define CV(x) decltype(x)::value
template <int N, int D, typename L, typename C> DI void pipe(L&& load, C&& comp) {
  sfor<0, D>(load);
  SB();
  sfor<0, N>(LAM(auto i) {
    comp(i);
    if constexpr (CV(i) + D < N) load(IC(CV(i) + D));
    SB();
  });
}
DI u32 lane_id() { u32 l; asm volatile("v_mbcnt_lo_u32_b32 %0, -1, 0\n\tv_mbcnt_hi_u32_b32 %0, -1, %0" : "=v"(l)); return l; }
#define LANE_VARS(x) [[maybe_unused]] const u32 l = (x), hf = l >> 5, t32 = l & 31, t16 = l & 15, g4 = l >> 4, trq = (l >> 2) & 3, trp = l & 3, blk = (l >> 4) & 1, vrow8 = l >> 3, vpos = l & 7
DI void rawbar() { asm volatile("s_barrier" ::: "memory"); }
constexpr u32 sp_slot(int s) { return s < 2 ? L_SPA + s * 4096 : L_SPB + (s - 2) * 4096; }

enum { K_SP, K_TAKE, K_ZP, K_WO, K_OTH };
struct Ev { int tick, kind, c, s, q; };
struct Sched { Ev e[400]; int n; };
constexpr int T_S2 = 0, T_Z2 = 10, T_W2 = 100, T_W3 = 200, T_W5 = 300, T_P3 = 1000;
constexpr int ZS_LD = 28;
constexpr int Z1_SPLIT = 40;
constexpr int Z_SPEC = 5;
constexpr int G_OUT[2] = {66, 130}, G_IN[2] = {146, 194}, G_USE[2] = {173, 229}, Z_PR2 = 34, Z_CK2 = 61;
constexpr int Y_ASK = 162, Y_CK = 190, Y_OUT = 196, Y_IN = 236, Y_PRE = 10;
constexpr Sched make_sched() {
  Sched S{};
  int n = 0;
  auto add = [&](int t, int k, int c, int s, int q) { S.e[n] = Ev{t, k, c, s, q}; ++n; };
  auto sp = [&](int t, int dt2, int c, int s) { for (int q = 0; q < 4; ++q) add(t + (q * dt2) / 2, K_SP, c, s, q); };
  auto take = [&](int t, int c, int s) { add(t, K_TAKE, c, s, -1); };
  auto take1 = [&](int t, int c, int s) { for (int q = 0; q < 4; ++q) add(t + q, K_TAKE, c, s, q); };
  sp(T_S2, 0, 0, 0); sp(T_S2, 0, 1, 1);
  take1(T_Z2 + 1, 0, 0); take1(T_Z2 + 5, 1, 1); sp(T_Z2 + 39, 4, 2, 0); sp(T_Z2 + 47, 4, 3, 1);
  sp(T_W2, 1, 4, 2); sp(T_W2 + 3, 1, 5, 3); sp(T_W2 + 6, 1, 6, 4); take(T_W2 + 8, 2, 0); sp(T_W2 + 10, 1, 7, 0); take(T_W2 + 11, 3, 1); sp(T_W2 + 13, 1, 8, 1);
  take(T_W3 + 1, 4, 2); sp(T_W3 + 6, 2, 9, 2); take(T_W3 + 7, 5, 3); sp(T_W3 + 12, 2, 10, 3); take(T_W3 + 13, 6, 4);
  sp(T_W3 + 18, 2, 11, 4); take1(T_W3 + 19, 7, 0); take1(T_W3 + 24, 8, 1); take1(T_W3 + 36, 9, 2);
  sp(T_W3 + ZS_LD + 8, 2, 12, 0); sp(T_W3 + Z1_SPLIT - 1, 0, 13, 1);
  take1(T_W3 + Z1_SPLIT, 10, 3); sp(T_W3 + 44, 4, 14, 2); take1(T_W3 + Z1_SPLIT + 4, 11, 4); take1(T_W3 + Z1_SPLIT + 8, 12, 0); take1(T_W3 + Z1_SPLIT + 12, 13, 1);
  for (int n2 = 0; n2 < 12; ++n2) add(T_W5 + Z_SPEC + n2 / 2, K_ZP, n2 >> 2, 0, n2 & 3);
  sp(T_W5 + Z_SPEC + 7, 2, 15, 3);
  take1(T_P3 + 36, 14, 2); take1(T_P3 + 40, 15, 3);
  for (int st = 1; st + 2 < 16; ++st) for (int q = 0; q < 4; ++q) add(T_P3 + 16 * st + 1 + q, K_ZP, st + 2, 0, q);
  const int wst[7] = {3, 5, 6, 7, 9, 13, 14};
  for (int c = 0; c < 7; ++c) for (int q = 0; q < 4; ++q) add(T_P3 + 16 * wst[c] + (c < 6 ? 9 : 1) + 2 * q, K_WO, c, 0, q);
  add(T_W2 + 16, K_OTH, 0, 0, 0);
  for (int i = 0; i < 8; ++i) add(T_W3 + ZS_LD + i, K_OTH, 1, 0, 0);
  for (int i = 0; i < 4; ++i) add(T_W3 + Z1_SPLIT - 1, K_OTH, 2, 0, 0);
  add(T_W5 + Z_SPEC - 1, K_OTH, 3, 0, 0);
  add(T_P3 + Z_PR2 + 3, K_OTH, 4, 0, 0);
  for (int p = 0; p < 2; ++p) {
    add(T_P3 + Y_OUT + 2 * p + 5, K_OTH, 8, 0, 0);
    for (int i = 0; i < 2; ++i) {
      add(T_P3 + G_OUT[p] + 4 * i + 6, K_OTH, 8, 0, 0);
      add(T_P3 + G_IN[p] + i + 3, K_OTH, 6 + p, 0, 0);
    }
  }
  add(T_P3 + Y_ASK + 3, K_OTH, 15, 0, 0);
  for (int i = 0; i < Y_PRE; ++i) add(T_P3 + Y_IN + 2 * i + 3, K_OTH, 9, 0, 0);
  S.n = n;
  return S;
}
constexpr Sched SCH = make_sched();
constexpr bool no_ties() {
  for (int i = 0; i < SCH.n; ++i) for (int k = 0; k < i; ++k)
    if (SCH.e[i].kind == K_OTH && SCH.e[k].kind == K_OTH && SCH.e[i].tick == SCH.e[k].tick && SCH.e[i].c != SCH.e[k].c) return false;
  return true;
}
static_assert(ZS_LD + 12 <= Z1_SPLIT && Z_SPEC + 10 < 16 && no_ties() && G_IN[0] > G_OUT[1] + 9 && G_IN[1] > G_USE[0] + 18 && Y_OUT > G_USE[0] + 18 && G_USE[1] > Y_OUT + 4 && G_USE[1] + 18 < 256 && Z_CK2 + 2 < G_OUT[0] && Y_CK + 2 < Y_OUT, "schedule");
constexpr bool is_piece(const Ev& e) { return e.kind != K_TAKE; }
constexpr int ev_rank(int i) { return SCH.e[i].tick * 512 + i; }
constexpr int younger(int kind, int c, int pos) {
  int last = -1;
  for (int i = 0; i < SCH.n; ++i) if (SCH.e[i].kind == kind && SCH.e[i].c == c && ev_rank(i) > last) last = ev_rank(i);
  int n = 0;
  for (int i = 0; i < SCH.n; ++i) if (is_piece(SCH.e[i]) && ev_rank(i) > last && ev_rank(i) < pos) ++n;
  return n;
}
constexpr bool needs_m0(int i) {
  if (SCH.e[i].q == 0) return true;
  int prev = -1;
  for (int k = 0; k < SCH.n; ++k) if (is_piece(SCH.e[k]) && SCH.e[k].kind != K_OTH && ev_rank(k) < ev_rank(i) && (prev < 0 || ev_rank(k) > ev_rank(prev))) prev = k;
  return SCH.e[prev].kind != SCH.e[i].kind || SCH.e[prev].c != SCH.e[i].c;
}
constexpr bool m0_ahead(int i) {
  if (SCH.e[i].tick <= T_P3) return false;
  for (int k = 0; k < i; ++k) if (SCH.e[k].tick == SCH.e[i].tick && SCH.e[k].kind != K_TAKE && SCH.e[k].kind != K_OTH) return false;
  return true;
}
constexpr int ev_first(int tick) { for (int i = 0; i < SCH.n; ++i) if (SCH.e[i].tick == tick) return i; return -1; }
constexpr int ev_next(int i) { for (int k = i + 1; k < SCH.n; ++k) if (SCH.e[k].tick == SCH.e[i].tick) return k; return -1; }
template <int I, typename F> DI void run_evs(F&& f) {
  if constexpr (I >= 0) {
    f(IC(I));
    run_evs<ev_next(I)>(f);
  }
}

__global__ void __launch_bounds__(256, 1)
fused_kernel(const void* __restrict__ x_, const void* __restrict__ wqkv_, const void* __restrict__ wo_, const void* __restrict__ win_,
             const void* __restrict__ wsp_, const void* __restrict__ bsp_, const void* __restrict__ wout_, void* __restrict__ out_, char* __restrict__ ws) {
  __shared__ __attribute__((aligned(1024))) char smem[LDS_BYTES];
  asm volatile("" ::"v"((u32)(uintptr_t)(LAS char*)smem));
  own_acc_file();
  const u32 tid = threadIdx.x, l = tid & 63, w = rfl(tid >> 6), wg = blockIdx.x;
  const u32 xcd = wg & 7, rr_ = wg >> 3;
  const u32 p1 = 8 * (xcd >> 1) + (rr_ & 7), jh = 4 * (xcd & 1) + (rr_ >> 3);
  const u32 j = rr_ >= 28 ? jh ^ 4 : jh;
  const u32 id = p1 * 8 + jh;
  const u32 P = p1 >> 1, g14 = 2 * j + (p1 & 1);
  const u32x4 sX = make_srd(x_, 2048 * 896), sWQKV = make_srd(wqkv_, 448 * 2688), sWS = make_srd(ws, WS_BYTES);
  const u32 zero = 0;
  constexpr u32 IDLE = 0xf0000000u;
  const u32x4 sent4 = {SENT, SENT, SENT, SENT};
  const u32 hh = w & 1, th = w >> 1, hf = l >> 5, t32 = l & 31, t16 = l & 15, g4 = l >> 4;
  const u32 trq = (l >> 2) & 3, trp = l & 3, blk = (l >> 4) & 1, vrow8 = l >> 3, vpos = l & 7;

  u32x4 xr[2][7];
  u32x2 epoch;
  {
    const u32 vo = (64 * p1 + 16 * w + vrow8) * 896 + vpos * 16;
    SFOR(i, 0, 7) ld128<i * 128, false>(xr[0][i], vo, sX, zero); SEND
    SFOR(i, 0, 7) ld128<i * 128, false>(xr[1][i], vo, sX, 8 * 896); SEND
    SB();
    const u32 n = (4 * rr_ + w) * 42 + l, row = (n * 5462) >> 16, r12 = n - 12 * row;
    touch((row * 2688 + (r12 >> 2) * 896 + ((r12 & 3) + 3 * (xcd & 1)) * 128) | (l < 42 ? 0 : IDLE), sWQKV, zero);
    SB();
    const u32 vc = (20 * rr_ + 5 * w + l) * 128 | (l < 5 ? 0 : IDLE);
    ld64<0, true>(epoch, zero, sWS, WS_EPOCH);
    u64 pc;
    u32 len;
    asm volatile("s_getpc_b64 %0\nfused_code_begin:\n\ts_mov_b32 %1, fused_code_end-fused_code_begin" : "=s"(pc), "=s"(len));
    touch(vc, u32x4{(u32)pc, (u32)(pc >> 32) & 0xffffu, len, 0x00020000u}, zero);
  }
  const u32 rho = 8 * w + vpos;
  const u32 oKVP = WS_KVP + ((jh * 32 + 2 * (8 * th + 2 * (vrow8 & 3) + hh) + hf) * 32 + p1) * 128 + vpos * 16;
  const u32 oPQ = WS_PQ + ((p1 * 4 + 2 * th + blk) * 16 + 2 * jh + hh) * 1024 + hf * 256 + t16 * 16;
  auto clear1 = LAM(auto i_) {
    constexpr int i = CV(i_);
    const u32 oKVC = WS_KVR + jh * 32768 + ((2 * vrow8 + (p1 & 1)) * 16 + (p1 >> 1)) * 128 + vpos * 16;
    if constexpr (i == 0) st128(sWS, oKVP, sent4);
    else if constexpr (i == 1) st128(sWS, w == 0 ? oKVC : (w == 1 && l < 16 && j == 7 && p1 < 28 ? WS_CS + p1 * 256 + l * 16 : IDLE), sent4);
    else st128(sWS, oPQ + (i - 2) * 512, sent4);
  };
  u32 vkv[8], vq[7];
  SFOR(i, 0, 8)
    const u32 c = 64 * i + l, R = (c * 9363) >> 16;
    vkv[i] = 16 * c + 784 * R + 896 * (R >> 1) + 896 + 112 * jh - 1024 * (i & 3);
    if constexpr (i < 7) vq[i] = 16 * c - 112 * R + (R >> 1) * 2688 + (R & 1) * (224 * 2688) + 112 * jh + 3072 - 1024 * (i & 3);
  SEND
  auto kv_piece = LAM(auto r_, auto i_) {
    constexpr int r = CV(r_), i = CV(i_);
    if constexpr ((i & 3) == 0) setm0(L_WKV + (4 * r + w) * 7168 + i * 1024);
    dman<(i & 3) * 1024>(vkv[i], sWQKV, (4 * r + w) * 32 * 2688);
  };
  auto kv_n = LAM(auto n_) {
    constexpr int n = CV(n_);
    if constexpr (n < 21) kv_piece(IC(n / 7), IC(n % 7));
    else {
      if constexpr (n == 21) setm0(L_WKV + (12 + th) * 7168 + hh * 4096);
      dman<(n - 21) * 1024>(hh ? vkv[4 + n - 21] : vkv[n - 21], sWQKV, (12 + th) * 32 * 2688);
    }
  };
  SFOR(r, 0, 2)
    waitvm<(r ? 7 : 10)>();

    SFOR(i, 0, 7) pin(xr[r][i]); SEND
    float s = 0.f, q = 0.f;
    SFOR(i, 0, 7)
      SFOR(e, 0, 4) s = dot2(xr[r][i][e], 0x3f803f80u, s); q = dot2(xr[r][i][e], xr[r][i][e], q); SEND
      if constexpr (r == 1) { if constexpr (i < 3) kv_n(IC(4 + i)); else clear1(IC(i - 3)); }
      SB();
    SEND
    s = dpp_add<0xB1>(s); q = dpp_add<0xB1>(q);
    s = dpp_add<0x4E>(s); q = dpp_add<0x4E>(q);
    s = dpp_add<0x141>(s); q = dpp_add<0x141>(q);
    const float mean = s * (1.0f / 448);
    const float rs = __builtin_amdgcn_rsqf(__builtin_fmaxf(q * (1.0f / 448) - mean * mean, 0.f) + 1e-5f), nm = -mean * rs;
    const u32 a = L_X + (16 * w + 8 * r + vrow8) * XSTR + vpos * 16;
    SFOR(i, 0, 7)
      u32x4 o;
      SFOR(e, 0, 2)
        const f32x2 t0 = __builtin_elementwise_fma(f32x2{bflo(xr[r][i][2 * e]), bfhi(xr[r][i][2 * e])}, f32x2{rs, rs}, f32x2{nm, nm});
        const f32x2 t1 = __builtin_elementwise_fma(f32x2{bflo(xr[r][i][2 * e + 1]), bfhi(xr[r][i][2 * e + 1])}, f32x2{rs, rs}, f32x2{nm, nm});
        o[e] = cvtpk(t0.x, t1.x);
        o[2 + e] = cvtpk(t0.y, t1.y);
      SEND
      *(LAS u32x4*)(a + i * 128) = o;
      if constexpr (r == 0 && ((i & 1) || i == 6)) kv_n(IC(i / 2));
      if constexpr (r == 1) kv_n(IC(7 + i));
      SB();
    SEND
  SEND
  const u32x4 sWQKV2 = make_srd((const char*)wqkv_ - 3072, 448 * 2688 + 3072);
  u32 aX = L_X + (32 * th + t32) * XSTR + hf * 16;
  u64 tag;
  auto unset = LAM(const u32x2& f, u64 lanes) { return (lanes_ne(f, tag) & lanes) != 0; };
  const u32x4 sWIN = make_srd(win_, 448 * 3584), sWO = make_srd(wo_, 448 * 896);
  u32x4 xt[2];
  u32x2 ini;
  const u32 vi = tid < 40 ? (l < 32 ? l * 8 + jh : p1 * 8 + (l & 7)) * 128 : IDLE;
  auto q_piece = LAM(auto n_) {
    constexpr int u = CV(n_) / 7, i = CV(n_) % 7;
    if constexpr ((i & 3) == 0) setm0(L_WQ + (4 * u + w) * 7168 + i * 1024);
    dman<(i & 3) * 1024>(vq[i], sWQKV2, u == 1 && w == 3 ? 448 * 2688 : (4 * u + w) * 32 * 2688);
  };
  {
    f32x16 ak, av;
    u32 aW[2];
    aW[0] = L_WKV + (8 * hf + 2 * trq) * 224 + 56 * hh + 32 * blk + 8 * trp;
    aW[1] = aW[0] + 256 * 224;
    pin(aW[0]); pin(aW[1]);
    u32x4 fk[2], fv[2];
    pipe<28, 2>(LAM(auto k_) {
      constexpr int ks = CV(k_), s = ks >> 3, r = ks & 7;
      if constexpr (r == 0) {
        constexpr int nv[4] = {11, 8, 7, 6};
        waitvm<nv[s]>();
        bar();
        if constexpr (s == 1) {
          pin(epoch);
          tag = (((u64)rfl(epoch.y) << 32) | rfl(epoch.x)) + 1;
          st64(sWS, tid == 0 ? WS_INIT + id * 128 : 0xfffffff0u, u32x2{(u32)tag, (u32)(tag >> 32)});
        }
      }
      if constexpr (ks < 11) kv_n(IC(14 + ks));
      else if constexpr (!(ks & 1) && ks < 26) q_piece(IC((ks - 12) / 2));
      constexpr int o = (16 * (ks & 15)) * 224;
      take1<4 * ks, 32 * ks>(aX);
      fk[ks & 1] = trfrag(aW[ks >> 4] + o, aW[ks >> 4] + o + 224);
      fv[ks & 1] = trfrag(aW[ks >> 4] + o + 112, aW[ks >> 4] + o + 224 + 112);
    }, LAM(auto k_) {
      constexpr int ks = CV(k_);
      if constexpr (ks == 0) { mm32Az<0>(ak, fk[0]); mm32Az<0>(av, fv[0]); }
      else { mm32A<4 * ks>(ak, fk[ks & 1]); mm32A<4 * ks>(av, fv[ks & 1]); }
    });
    SFOR(i, 0, 2) ld128<0, false>(xt[i], (64 * p1 + 16 * w + vrow8) * 896 + 128 * j + vpos * 16, sX, i * 8 * 896); SEND
    ld64<0, true>(ini, vi, sWS, WS_INIT);
    touch(l < 14 ? ((p1 & 7) * 56 + 14 * w + l) * 896 + 128 * (j == 7 ? 3 : j) : IDLE, sWO, zero);
    {
      const u32 u = 4 * rr_ + w, c = (u & 7) + 8 * (xcd & 1);
      touch(l < 14 && c < 14 ? ((xcd >> 1) * 112 + (u >> 4) * 14 + l) * 3584 + ((u >> 3) & 1) * 1792 + 128 * c : IDLE, sWIN, zero);
    }
    nops<12>(ak, av);
    u32x4 pp[4];
    SFOR(i, 0, 8)
      pp[i >> 2][i & 3] = cvtpk(phi(ak[2 * i]), phi(ak[2 * i + 1]));
      pp[2 + (i >> 2)][i & 3] = t32 == 28 ? 0x3f803f80u : cvtpk(av[2 * i], av[2 * i + 1]);
      if constexpr (i & 1) { pin(pp[i >> 2]); pin(pp[2 + (i >> 2)]); q_piece(IC(7 + (i >> 1))); SB(); }
    SEND
    nops4<2>(pp);
    f32x16 kv;
    mm32z(kv, pp[2], pp[0]);
    mm32(kv, pp[3], pp[1]);
    nops<12>(kv);
    const u32 a = L_KST + (32 * w + 4 * hf) * KSTR + t32 * 4;
    SFOR(i, 0, 16) *(LAS float*)(a + ((i & 3) + 8 * (i >> 2)) * KSTR) = kv[i]; SEND
  }
  waitpin<6>(xt[0]);
  pin(xt[1]); pin(ini);
  bar();
  {
    const u32 e = rho < 16 ? rho : ((rho & 3) == 3 ? 28u : 16 + 3 * ((rho - 16) >> 2) + (rho & 3));
    const u32 a = L_KST + (32 * hf + e) * KSTR + blk * 64 + (vrow8 & 1) * 16;
    const f32x4 u = lds128f(a) + lds128f(a + 64 * KSTR), v = lds128f(a + 32) + lds128f(a + 32 + 64 * KSTR);
    st128(sWS, oKVP, u32x4{cvtpk(u[0], u[1]), cvtpk(u[2], u[3]), cvtpk(v[0], v[1]), cvtpk(v[2], v[3])});
  }
  sfor<11, 14>(q_piece);
  const u32x4 sWSP = make_srd(wsp_, 2048 * 4096), sWOUT = make_srd(wout_, 896 * 896);
  u32 vz[4];
  SFOR(q, 0, 4) vz[q] = vrow8 * 3584 + (vpos ^ (blk << 2)) * 16 + 128 * g14 + q * (8 * 3584 - 1024); SEND
  auto wz_src = LAM(u32 n) { n += 8 * j; return (n >= 56 ? n - 56 : n) * 8 * 3584; };
  auto wz_piece = LAM(auto i_, auto g_, u32 first, u32 lds, u32 src) {
    constexpr int i = CV(i_), q = i % CV(g_);
    if constexpr (q == 0) setm0(lds + (first + i) * 1024);
    dman<q * 1024>(vz[q], sWIN, src);
  };
  const u32 oH = WS_H + ((2 * p1 + th) * 28 + 4 * j + (g4 >> 1)) * 1024 + (g4 & 1) * 512 + (16 * hh + t16) * 16;
  const u32 oHST = WS_HST + ((P * 7 + j) * 128 + 64 * (p1 & 1) + 16 * w + l) * 8;
  const u32 oZST = WS_ZST + ((P * 14 + g14) * 128 + 32 * w + l) * 8;
  f32x4 hacc[4];
  u32 aO[2][4];
  auto h_clear = LAM() {
    st128(sWS, oH, sent4); st128(sWS, oH + 2048, sent4);
    st64(sWS, l < 16 ? oHST : 0xfffffff0u, u32x2{SENT, SENT});
    st64(sWS, l < 32 ? oZST : 0xfffffff0u, u32x2{SENT, SENT});
  };
  {
    f32x16 aq;
    u32 aW = L_WQ + (8 * hf + 2 * trq) * 224 + 56 * hh + 32 * blk + 8 * trp;
    pin(aW);
    u32x4 fq[4];
    u32x2 xs[4];
    const u32 aT = L_XT + w * 2304;
    auto qgemm = LAM(auto&& extra) {
      pipe<28, 4>(LAM(auto k_) {
        constexpr int ks = CV(k_), o = (16 * (ks >> 1)) * 224 + (ks & 1) * 112;
        if constexpr (ks == 16) { waitvm<0>(); rawbar(); }
        fq[ks & 3] = trfrag(aW + o, aW + o + 224);
      }, LAM(auto k_) {
        constexpr int ks = CV(k_);
        if constexpr (ks == 0) mm32az<0>(aq, fq[0]); else mm32a<4 * ((ks >> 1) + 14 * (ks & 1))>(aq, fq[ks & 3]);
        if constexpr (ks < 2) *(LAS u32x4*)(aT + (8 * ks + vrow8) * 144 + vpos * 16) = xt[ks];
        if constexpr (ks >= 4 && ks < 8) xs[ks - 4] = *(LAS u32x2*)(aT + t16 * 144 + (ks - 4) * 32 + g4 * 8);
        if constexpr (ks >= 10 && ks < 14) { hacc[ks - 10] = f32x4{bflo(xs[ks - 10].x), bfhi(xs[ks - 10].x), bflo(xs[ks - 10].y), bfhi(xs[ks - 10].y)}; pin(hacc[ks - 10]); }
        if constexpr (ks >= 16 && ks < 24) {
          constexpr int y = ks & 3;
          const u32 r = ks < 20 ? 4 * g4 + trq : 16 + 3 * g4 + (trq < 2 ? trq : 2);
          aO[(ks >> 2) & 1][y] = L_WO + (112 * w + r) * 128 + ((y ^ ((r >> 1) & 3)) * 32) + 8 * trp;
          pin(aO[(ks >> 2) & 1][y]);
        }
        extra(k_);
      });
    };
    u32x4 pq[2];
    auto phiq = LAM() {
      nops<12>(aq);
      SFOR(i, 0, 8)
        const u32 a = cvtpk(phi(aq[2 * i]), phi(aq[2 * i + 1]));
        pq[i >> 2][i & 3] = (i >= 6 && hf) ? 0u : a;
      SEND
      pin(pq[0]); pin(pq[1]);
    };
    auto phiq_out = LAM() { st128(sWS, oPQ, pq[0]); st128(sWS, oPQ + 512, pq[1]); };
    if (w == 0) {
      u32x4 pr[4];
      const u32 vkp = WS_KVP + (jh * 32 + p1) * 4096 + l * 16;
      auto kvp_load = LAM() { SFOR(i, 0, 4) ld128<i * 1024, true>(pr[i], vkp, sWS, zero); SEND };
      qgemm(LAM(auto k_) {});
      if (unset(ini, 0xffffffffffull)) {
        u32 spins = 0;
        do {
          ld64<0, true>(ini, vi, sWS, WS_INIT);
          waitpin<0>(ini);
        } while (unset(ini, 0xffffffffffull) && !giveup(spins, ws, tag));
      }
      kvp_load();
      phiq();
      rawbar();
      f32x2 s[4];
      auto kvp_bad = LAM() {
        waitvm<0>();
        SFOR(i, 0, 4)
          pin(pr[i]);
          SFOR(e, 0, 4) const f32x2 t = {bflo(pr[i][e]), bfhi(pr[i][e])}; if constexpr (i == 0) s[e] = t; else s[e] += t; SEND
        SEND
        const f32x2 t = (s[0] + s[1]) + (s[2] + s[3]);
        return __any(isnan_(t.x + t.y));
      };
      if (kvp_bad()) {
        u32 spins = 0;
        do kvp_load(); while (kvp_bad() && !giveup(spins, ws, tag));
      }
      u32x4 t;
      SFOR(e, 0, 4) t[e] = cvtpk(add_x32(add_x16(dpp_add<0x128>(s[e].x))), add_x32(add_x16(dpp_add<0x128>(s[e].y)))); SEND
      st128(sWS, WS_KVR + vrow8 * 32768 + ((2 * jh + (p1 & 1)) * 16 + (p1 >> 1)) * 128 + vpos * 16, t);
      phiq_out();
      rawbar();
    } else if (j < 7) {
      u32 vo4[4];
      SFOR(q, 0, 4) vo4[q] = vrow8 * 896 + (vpos ^ (g4 << 1)) * 16 + 128 * j + q * (8 * 896 - 1024); SEND
      auto wo_piece = LAM(auto i_) {
        constexpr int i = CV(i_);
        if constexpr ((i & 3) == 0) setm0(L_WO + (19 * (w - 1) + i) * 1024);
        dman<(i & 3) * 1024>(vo4[i & 3], sWO, (19 * (w - 1) + (i & ~3)) * 8 * 896);
      };
      qgemm(LAM(auto k_) { if constexpr (CV(k_) >= 13) wo_piece(IC(CV(k_) - 13)); });
      phiq();
      phiq_out();
      rawbar();
      sfor<15, 19>(wo_piece);
      const u32 first = min(20 * (w - 1), 36u);
      SFOR(i, 0, 20) wz_piece(IC(i), IC(4), first, L_WZ2, wz_src(first + (i & ~3)) + 1792); SEND
      waitvm<20>();
      rawbar();
    } else {
      qgemm(LAM(auto k_) {});
      phiq();
      phiq_out();
      rawbar();
      rawbar();
    }
  }
  if (j == 7) {
    u32x4 pr[14];
    const u32 vo = p1 < 28 ? (112 * w + vrow8) * 3584 + vpos * 16 + 128 * p1 : IDLE;
    SFOR(i, 0, 14) ld128<0, false>(pr[i], vo, sWIN, i * 8 * 3584); SEND
    auto sp_touch = LAM(auto n_) { touch((128 * P + 32 * w + t32) * 4096 + hf * 128, sWSP, (8 * (p1 & 1) + ((P + CV(n_)) & 7)) * 256); };
    sfor<0, 2>(sp_touch);
    waitvm<2>();
    {
      f32x2 c[4] = {};
      SFOR(i, 0, 14) pin(pr[i]); SFOR(e, 0, 4) c[e] += f32x2{bflo(pr[i][e]), bfhi(pr[i][e])}; SEND SEND
      float cs[8];
      SFOR(e, 0, 8) cs[e] = add_x32(add_x16(dpp_add<0x128>(c[e >> 1][e & 1]))); SEND
      if (l < 8) {
        *(LAS f32x4*)(L_MISC + w * 256 + l * 32) = f32x4{cs[0], cs[1], cs[2], cs[3]};
        *(LAS f32x4*)(L_MISC + w * 256 + l * 32 + 16) = f32x4{cs[4], cs[5], cs[6], cs[7]};
      }
      bar();
      if (tid < 16 && p1 < 28) {
        const u32 a = L_MISC + l * 16;
        st128(sWS, WS_CS + p1 * 256 + l * 16, __builtin_bit_cast(u32x4, (lds128f(a) + lds128f(a + 256)) + (lds128f(a + 512) + lds128f(a + 768))));
      }
    }
    sfor<2, 8>(sp_touch);
    SFOR(kind, 0, 2)
      for (u32 c = 0; c < 7; ++c) {
        const u32 o = l * 16 + (kind == 0 ? WS_G + (P * 14 + 2 * c + (p1 & 1)) * 16384 + w * 4096 : WS_Y + (4 * p1 + w) * 28672 + c * 4096);
        SFOR(q, 0, 4) st128(sWS, o + q * 1024, sent4); SEND
      }
      waitvm<0>();
      rawbar();
      st64(sWS, tid == 0 ? WS_FLG + kind * 4096 + p1 * 128 : IDLE, u32x2{(u32)tag, (u32)(tag >> 32)});
      if constexpr (kind == 0) touch(l < 49 ? (p1 * 196 + 49 * w + l) * 128 : IDLE, sWOUT, zero);
    SEND
    return;
  }
  u32 bbits, wz1[7];
  SFOR(c, 0, 7) wz1[c] = wz_src(14 * w + 2 * c); spin(wz1[c]); SEND
  u32x2 fl;
  const u32 vfh = t32 < 14 ? WS_FLH + 128 * (P * 4 + (hf ? t32 * 64 + w : ((t32 & 14) + th) * 64 + 2 * hh + (l & 1))) : IDLE;
  const u32 kh = g14 & 1;
  {
    u32x4 pq[16], kf[8];
    const u32 vp = WS_PQ + (p1 * 64 + 4 * w) * 1024 + l * 16;
    const u32 vk = WS_KVR + jh * 32768 + w * 8192 + l * 16;
    auto pq_load = LAM(auto h_) { constexpr int h = CV(h_); ld128<(h & 3) * 1024, true>(pq[h], vp, sWS, ((w + 1 + (h >> 2)) & 3) * 16384); };
    auto kf_load = LAM() { SFOR(i, 0, 8) ld128<(i & 3) * 1024, true>(kf[i], vk, sWS, (i >> 2) * 4096); SEND };
    SFOR(h, 0, 4) SFOR(n, 0, 4) taketr<128 + 16 * h + 4 * n, h * 3584, h * 3584>(aO[0][n ^ (2 * (h & 1))], aO[1][n ^ (2 * (h & 1))]); SEND SEND
    SFOR(h, 0, 8) pq_load(IC(2 * h + 1)); SEND
    bar();
    SFOR(h, 0, 8) pq_load(IC(2 * h)); SEND
    kf_load();
    h_clear();
    const u32 oZc = WS_Z + (g14 * 16 + P) * 16384 + w * 4096 + l * 16;
    auto pq_bad = LAM(auto e_) {
      u32 m = 0;
      SFOR(h2, 0, 8) constexpr int h = 2 * h2 + CV(e_); pin(pq[h]); m = umax3(umax3(m, pq[h].x, pq[h].y), pq[h].z, pq[h].w); SEND
      return __any(m == SENT);
    };
    auto kf_bad = LAM() {
      u32 m = 0;
      SFOR(i, 0, 8) pin(kf[i]); m = umax3(umax3(m, kf[i].x, kf[i].y), kf[i].z, kf[i].w); SEND
      return __any(m == SENT);
    };
    waitvm<20>();
    const bool pq1 = pq_bad(IC(1));
    waitvm<12>();
    const bool pq0 = pq_bad(IC(0));
    waitvm<4>();
    if (kf_bad() || pq0) {
      u32 spins = 0;
      do {
        SFOR(i, 0, 8)
          if (lanes_eq(max(umax3(pq[2 * i].x, pq[2 * i].y, pq[2 * i].z), pq[2 * i].w), SENT) != 0) pq_load(IC(2 * i));
          if (lanes_eq(max(umax3(kf[i].x, kf[i].y, kf[i].z), kf[i].w), SENT) != 0) ld128<(i & 3) * 1024, true>(kf[i], vk, sWS, (i >> 2) * 4096);
        SEND
        waitvm<0>();
      } while ((pq_bad(IC(0)) | kf_bad()) && !giveup(spins, ws, tag));
    }
    if (pq1) {
      u32 spins = 0;
      do { SFOR(h, 0, 8) pq_load(IC(2 * h + 1)); SEND waitvm<0>(); } while (pq_bad(IC(1)) && !giveup(spins, ws, tag));
    }
    const u32 oFL = l == 0 ? ((g14 * 16 + P) * 4 + w) * 128 : 0xff000000u;
    u32 aR = L_RED + l * 16 + w * 12288, aRo[3];
    SFOR(t, 0, 3) aRo[t] = L_RED + l * 16 + ((w + 1 + t) & 3) * 12288 + t * 4096; SEND
    u32x4 ob[2];
    float m[7];
    f32x4 d[2][2], pa[2][4], rd[3][4], eps = {0.f, 0.f, 0.f, 1e-6f};
    pin(eps);
    SB();
    SFOR(st, 0, 18)
      constexpr int h1 = st - 1, h2 = st - 2, R = 128 + 16 * (h2 & 3), t2 = h2 >> 2, tw = (st - 7) >> 2;
      constexpr bool V = st >= 1 && st < 17, M = st >= 2;
      auto wo_mm = LAM(auto n_) {
        constexpr int n = CV(n_);
        if constexpr (M) {
          if constexpr (t2 == 3) mm16a<R + 4 * n>(hacc[n], ob[h2 & 1]);
          else if constexpr ((h2 & 3) == 0) mm16az<R + 4 * n>(pa[t2 & 1][n], ob[h2 & 1]);
          else mm16a<R + 4 * n>(pa[t2 & 1][n], ob[h2 & 1]);
        }
        SB();
      };
      float r;
      if constexpr (V) { pin(d[h1 & 1][0]); pin(d[h1 & 1][1]); }
      if constexpr (M) ob[h2 & 1][2] = cvtpk(m[4], m[5]);
      SB();
      if constexpr (V) r = __builtin_amdgcn_rcpf(d[h1 & 1][1][3]);
      SB();
      if constexpr (M) ob[h2 & 1][3] = cvtpk(m[6], 0.f);
      SB();
      if constexpr (st < 16) mm16c2(d[st & 1][0], d[st & 1][1], kf[2 * (st & 3)], kf[2 * (st & 3) + 1], pq[st], eps);
      if constexpr (st == 0) nops<8>(d[0][0], d[0][1]);
      if constexpr (st == 17) nops<2>(ob[1]);
      SB();
      if constexpr (V) { SFOR(e, 0, 2) m[e] = d[h1 & 1][0][e] * r; pin(m[e]); SEND }
      SB();
      wo_mm(IC(0));
      if constexpr (V) { SFOR(e, 2, 4) m[e] = d[h1 & 1][0][e] * r; pin(m[e]); SEND SB(); ob[h1 & 1][0] = cvtpk(m[0], m[1]); }
      SB();
      wo_mm(IC(1));
      if constexpr (V) { SFOR(e, 4, 7) m[e] = d[h1 & 1][1][e - 4] * r; pin(m[e]); SEND }
      SB();
      wo_mm(IC(2));
      if constexpr (V) ob[h1 & 1][1] = cvtpk(m[2], m[3]);
      SB();
      wo_mm(IC(3));
      if constexpr (st >= 7 && st < 17 && ((st - 7) & 3) < 2) {
        SFOR(n, 2 * ((st - 7) & 3), 2 * ((st - 7) & 3) + 2) pin(pa[tw & 1][n]); *(LAS f32x4*)(aRo[tw] + n * 1024) = pa[tw & 1][n]; SEND
      }
      if constexpr (st < 4) st128(sWS, oZc + st * 1024, sent4);
      if constexpr (st == 6) {
        waitvm<9>();
        st64(sWS, oFL + WS_FLH, u32x2{(u32)tag, (u32)(tag >> 32)});
      }
      if constexpr (st == 1) ld16(bbits, (128 * P + 32 * w + t32) * 2, make_srd(bsp_, 4096));
      if constexpr (st >= 2 && st < 16) wz_piece(IC(st - 2), IC(2), 14 * w, L_WZ1, wz1[(st - 2) >> 1]);
      if constexpr (st == 16) {
        bar();
        SFOR(t, 0, 3) SFOR(n, 0, 4) rd[t][n] = lds128f(aR + t * 4096 + n * 1024); SEND SEND
        ld64<0, true>(fl, vfh, sWS, zero);
      }
      SB();
    SEND
    SFOR(n, 0, 4) rd[0][n] += rd[1][n] + rd[2][n]; pin(rd[0][n]); SEND
    SB();
    nops4<2>(hacc);
    SFOR(n, 0, 4) hacc[n] += rd[0][n]; SEND
    SFOR(m, 0, 2)
      st128(sWS, oH + m * 2048, u32x4{cvtpk(hacc[2 * m][0], hacc[2 * m][1]), cvtpk(hacc[2 * m][2], hacc[2 * m][3]), cvtpk(hacc[2 * m + 1][0], hacc[2 * m + 1][1]),
                                      cvtpk(hacc[2 * m + 1][2], hacc[2 * m + 1][3])});
    SEND
    f32x2 s2 = {0.f, 0.f}, q2 = {0.f, 0.f};
    SFOR(n, 0, 8)
      const f32x2 t = {hacc[n >> 1][2 * (n & 1)], hacc[n >> 1][2 * (n & 1) + 1]};
      s2 += t;
      q2 = __builtin_elementwise_fma(t, t, q2);
    SEND
    const float s = add_x32(add_x16(s2.x + s2.y)), q = add_x32(add_x16(q2.x + q2.y));
    st64(sWS, l < 16 ? oHST : 0xfffffff0u, u32x2{asu(s), asu(q)});
  }
  waitvm<3>();
  st64(sWS, l == 0 ? WS_FLZ + ((g14 * 16 + P) * 4 + w) * 128 : IDLE, u32x2{(u32)tag, (u32)(tag >> 32)});
  bar();
  if (wg == 0 && tid == 0) __hip_atomic_store((GAS u64*)(ws + WS_EPOCH), tag, RLX_AGENT);
  u32 vS[2][4], aS[2][4], vO[4];
  SFOR(q, 0, 4)
    vS[0][q] = (128 * P + 32 * w + vrow8) * 4096 + (vpos ^ (4 * (q & 1) + g4)) * 16 + q * (8 * 4096 - 1024);
    vS[1][q] = vS[0][q] + 128;
    const u32 b = t32 * 128 + (((2 * q + hf) ^ ((t32 >> 1) & 7)) * 16);
    aS[0][q] = L_SPA + w * 8192 + b;
    aS[1][q] = L_SPB + w * 12288 + b;
  SEND
  u32 vZ[2][3], rz[8], vprobe;
  u32 pr1, vy;
  u32 rot[8], mw[4] = {0, w * 4096, w * 8192, w * 12288}, sO;
  SFOR(c, 0, 8) rot[c] = (8 * kh + ((P + c) & 7)) * 256; spin(rot[c]); SEND
  SFOR(c, 1, 4) spin(mw[c]); SEND
  auto bg_m0 = LAM(auto i_, auto nop_) {
    constexpr Ev e = SCH.e[CV(i_)];
    constexpr bool nop = CV(nop_);
    if constexpr (e.kind == K_SP) setm0<sp_slot(e.s), nop>(mw[e.s < 2 ? 2 : 3]);
    else if constexpr (e.kind == K_ZP) setm0<L_ZR + (e.c & 3) * 16384, nop>(mw[1]);
    else setm0<(e.c < 6 ? L_WOUT + 4 * e.c * 4096 : L_WOUT2), nop>(mw[1]);
  };
  auto bg_ev = LAM(auto i_) {
    constexpr int i = CV(i_);
    constexpr Ev e = SCH.e[i];
    if constexpr (e.kind == K_TAKE) {
      if constexpr (e.c >= 2 && e.q <= 0) waitvm<younger(K_SP, e.c, ev_rank(i))>();
      if constexpr (e.q < 0) take4<e.c * 16, (e.s < 2 ? e.s : e.s - 2) * 4096>(aS[e.s >= 2]);
      else take1<e.c * 16 + 4 * e.q, (e.s < 2 ? e.s : e.s - 2) * 4096>(aS[e.s >= 2][e.q]);
    } else if constexpr (e.kind != K_OTH) {
      if constexpr (e.kind == K_SP && e.q == 0 && e.tick >= T_W2 + 10 && e.tick < T_W3) waitlds();
      if constexpr (needs_m0(i) && !m0_ahead(i)) bg_m0(i_, IC(1));
      if constexpr (e.kind == K_SP) dman<e.q * 1024>(vS[e.c & 1][e.q], sWSP, rot[e.c >> 1]);
      if constexpr (e.kind == K_ZP) dman<e.q * 1024, true>(vZ[e.c >> 3][((e.c >> 2) & 1) + (e.q & 1)], sWS, rz[2 * (e.c & 3) + (e.q >> 1)]);
      if constexpr (e.kind == K_WO) {
        if constexpr (e.q == 0) { if constexpr (e.c == 0) sO = w * 32 * 896; else sO += 128 * 896; spin(sO); }
        dman<e.q * 1024>(vO[e.q], sWOUT, sO);
      }
    }
  };
#define BGM(t) run_evs<ev_first(t)>(LAM(auto i_) { if constexpr (SCH.e[CV(i_)].kind != K_TAKE && SCH.e[CV(i_)].kind != K_OTH) { if constexpr (needs_m0(CV(i_)) && m0_ahead(CV(i_))) bg_m0(i_, IC(0)); } })
#define BG(t) run_evs<ev_first(t)>(bg_ev)
  pin(bbits);
  BG(T_S2);
  pin(fl);
  if (unset(fl, 0x3fff00003fffull)) {
    u32 spins = 0;
    do { ld64<0, true>(fl, vfh, sWS, zero); waitpin<0>(fl); } while (unset(fl, 0x3fff00003fffull) && !giveup(spins, ws, tag));
  }
  u32x4 hb[28];
  u32x2 st[4];
  constexpr int NSPEC = 6, NAHEAD = 12, CS_LD = 2 * (20 - NAHEAD) + 1;
  const u32 vst = WS_HST + ((P * 7 + 4 * hf) * 128 + 32 * w + t32) * 8, vst3 = vst + (hf ? 0 : 3072);
  const u32 vh = WS_H + (4 * P + w) * 28 * 1024 + l * 16;
  u32 hs[7];
  SFOR(c, 0, 7) hs[c] = (c + j >= 7 ? c + j - 7 : c + j) * 4096; spin(hs[c]); SEND
  auto h_load = LAM() {
    SFOR(i, 0, 3) ld64<i * 1024, true>(st[i], vst, sWS, zero); SEND
    ld64<0, true>(st[3], vst3, sWS, zero);
    SFOR(i, 0, NSPEC) ld128<(i & 3) * 1024, true>(hb[i], vh, sWS, hs[i >> 2]); SEND
  };
  h_load();
  f32x2 z1v[16];
  float rs;
  {
    f32x16 z[2];
    float nmu;
    const u32 rowp = (4 * hf + trq) * 128 + blk * 32, pp = 8 * (2 * (trp & 1) + (trp >> 1));
    u32 aZ2[2] = {L_WZ2 + rowp + ((trq >> 1) * 64) + 8 * trp, L_WZ2 + rowp + (((trq >> 1) ^ 1) * 64) + 8 * trp};
    u32 aZ1[2] = {L_WZ1 + rowp + ((trq >> 1) * 64) + pp, L_WZ1 + rowp + (((trq >> 1) ^ 1) * 64) + pp};
    u32 aC = L_MISC + w * 512;
    pin(aZ2[0]); pin(aZ2[1]); pin(aZ1[0]); pin(aZ1[1]);
    u32x4 fa[4];
    f32x4 cq[2];
    float s, q;
    auto h_bad = LAM() {
      SFOR(i, 0, 4) pin(st[i]); SEND
      u32 m = 0;
      SFOR(i, 0, NSPEC) pin(hb[i]); m = max(m, hb[i].x); SEND
      s = asf(st[0].x) + asf(st[1].x) + asf(st[2].x) + (hf ? 0.f : asf(st[3].x));
      q = asf(st[0].y) + asf(st[1].y) + asf(st[2].y) + (hf ? 0.f : asf(st[3].y));
      return __any(isnan_(s + q) || m == SENT);
    };
    waitvm<0>();
    bool again = h_bad();
    for (u32 redo = 0;;) {
      if (again) {
        do { h_load(); waitvm<0>(); } while (h_bad() && !giveup(redo, ws, tag));
      }
      SFOR(i, NSPEC, NAHEAD) ld128<(i & 3) * 1024, true>(hb[i], vh, sWS, hs[i >> 2]); SEND
      s = add_x32(s) * (1.0f / 448);
      q = add_x32(q) * (1.0f / 448);
      rs = __builtin_amdgcn_rsqf(__builtin_fmaxf(q - s * s, 0.f) + 1e-5f);
      nmu = -s;
      u32 c1, c2;
      bool late;
      auto cs_load = LAM() {
        ld32<true>(c1, l * 4, sWS, WS_CS + g14 * 256);
        ld32<true>(c2, l * 4, sWS, WS_CS + (14 + g14) * 256);
      };
      pipe<56, 4>(LAM(auto i_) {
        constexpr int i = CV(i_), k = i >> 1, ro = (32 * (k >> 1) + 8 * (k & 1)) * 128;
        fa[i & 3] = trfrag(aZ2[i & 1] + ro, aZ2[i & 1] + ro + 2048);
      }, LAM(auto i_) {
        constexpr int i = CV(i_), k = i >> 1;
        if constexpr ((i & 1) == 0 && k >= NSPEC) waitpin<(k + NAHEAD <= 28 ? NAHEAD - 1 : 27 - k) + (i > 39 ? (i - 38) / 2 : 0) + (i > CS_LD && i <= 40 ? 2 : 0)>(hb[k]);
        if constexpr (i < 2) mm32z(z[i], fa[i], hb[0]); else mm32(z[i & 1], fa[i & 3], hb[k]);
        if constexpr ((i & 1) == 1 && k + NAHEAD < 28) ld128<((k + NAHEAD) & 3) * 1024, true>(hb[k + NAHEAD], vh, sWS, hs[(k + NAHEAD) >> 2]);
        if constexpr (i == CS_LD) cs_load();
        if constexpr (i == 42) { pin(c1); pin(c2); }
        if constexpr (i == 43) late = __any(isnan_(asf(c1) + asf(c2)));
        if constexpr (i == 44) {
          if (RARE(late)) {
            do { cs_load(); waitpin<0>(c1); pin(c2); } while (__any(isnan_(asf(c1) + asf(c2))) && !giveup(redo, ws, tag));
          }
          *(LAS u32*)(aC + l * 4) = c1;
          *(LAS u32*)(aC + 256 + l * 4) = c2;
        }
        BG(T_Z2 + i);
      });
      SFOR(i, 0, 2) cq[i] = lds128f(aC + 256 + hf * 16 + i * 32); SEND
      nops<12>(z[0], z[1]);
      if (!__any(isnan_(z[0][0])) || giveup(redo, ws, tag)) break;
      waitvm<0>();
      BG(T_S2);
      again = true;
    }
    rawbar();
    const float k1 = -2.0f * 0.7978845608028654f * LOG2E * rs, k3 = k1 * 0.044715f * rs * rs;
    f32x2 zz[16];
    {
      f32x2 s2 = {0.f, 0.f}, q2 = {0.f, 0.f};
      SFOR(i, 0, 16)
        BG(T_W2 + i);
        const f32x2 v = __builtin_elementwise_fma(f32x2{cq[(i >> 1) & 1][2 * (i & 1)], cq[(i >> 1) & 1][2 * (i & 1) + 1]}, f32x2{nmu, nmu}, f32x2{z[i >> 3][(2 * i) & 15], z[i >> 3][((2 * i) & 15) + 1]});
        if constexpr ((i & 1) && i < 13) cq[(i >> 1) & 1] = lds128f(aC + 256 + hf * 16 + ((i >> 1) + 2) * 32);
        zz[i] = gelu2(v, k1, k3);
        s2 += zz[i];
        q2 = __builtin_elementwise_fma(zz[i], zz[i], q2);
        if constexpr (i & 1) SB();
      SEND
      const float s = add_x32(s2.x + s2.y), q = add_x32(q2.x + q2.y);
      st64(sWS, l < 32 ? oZST : 0xfffffff0u, u32x2{asu(s), asu(q)});
    }
    SFOR(i, 0, 8)
      const f32x4 c = lds128f(aC + hf * 32 + (i >> 2) * 128 + ((i >> 1) & 1) * 64 + (i & 1) * 16) * nmu;
      SFOR(e, 0, 4) z[i >> 2][4 * (i & 3) + e] = c[e]; SEND
    SEND
    nops<2>(z[0], z[1]);
    u32x2 zs[8];
    const u32 vzs = WS_ZST + ((P * 14 + 7 * hf) * 128 + 32 * w + t32) * 8;
    const u32 vfl = l < 16 ? WS_FLZ + 128 * (((g14 ^ (l >> 3)) * 16 + 8 * kh + vpos) * 4 + w) : IDLE;
    auto zs_load = LAM(auto i_) { constexpr int i = CV(i_); ld64<(i & 3) * 1024 * (i < 7), true>(zs[i], i < 7 ? vzs : vfl, sWS, (i >> 2) * 4096 * (i < 7)); };
    auto z1_load = LAM(auto i_) {
      constexpr int i = CV(i_), k = i >> 1, ro = (32 * (k >> 1) + 8 * (k & 1)) * 128;
      fa[i & 3] = trfrag(aZ1[i & 1] + ro, aZ1[i & 1] + ro + 2048);
    };
    auto z1_step = LAM(auto i_) {
      constexpr int i = CV(i_);
      mm32(z[i & 1], fa[i & 3], hb[i >> 1]);
      BG(T_W3 + i);
      if constexpr (i >= ZS_LD && i < ZS_LD + 8) zs_load(IC(i - ZS_LD));
      if constexpr (i + 4 < 56) z1_load(IC(i + 4));
      SB();
    };
    sfor<0, 4>(z1_load);
    SB();
    sfor<0, Z1_SPLIT>(z1_step);
    LANE_VARS(lane_id());
    {
      float s, q;
      const u32 oZ = WS_Z + (g14 * 16 + P) * 16384 + w * 4096 + l * 16;
      auto zs_bad = LAM() {
        s = 0.f; q = 0.f;
        SFOR(i, 0, 7) pin(zs[i]); s += asf(zs[i].x); q += asf(zs[i].y); SEND
        pin(zs[7]);
        return __any(isnan_(s + q)) || unset(zs[7], 0xffff);
      };
      waitvm<8>();
      if (zs_bad()) {
        u32 spins = 0;
        do { sfor<0, 8>(zs_load); waitvm<0>(); } while (zs_bad() && !giveup(spins, ws, tag));
      }
      s = add_x32(s) * (1.0f / 896);
      q = add_x32(q) * (1.0f / 896);
      const float r2 = __builtin_amdgcn_rsqf(__builtin_fmaxf(q - s * s, 0.f) + 1e-5f * __builtin_amdgcn_rcpf(rs * rs)), n2 = -s * r2;
      const u32 aT = (w < 2 ? L_TRA + w * 4096 : L_TRB + (w - 2) * 4096);
      SFOR(c, 0, 4)
        u32x4 v;
        SFOR(e, 0, 4) const f32x2 t = __builtin_elementwise_fma(zz[4 * c + e], f32x2{r2, r2}, f32x2{n2, n2}); v[e] = cvtpk(t.x, t.y); SEND
        *(LAS u32x4*)(aT + c * 1024 + hf * 512 + (t32 ^ ((2 * (c & 1) + hf) << 2)) * 16) = v;
      SEND
      const u32 o3 = 2 * blk + (trp >> 1), b0 = aT + o3 * 512 + (((2 * hf) ^ o3) * 4 + trq) * 16 + (trp & 1) * 8;
      SFOR(c, 0, 4) st128(sWS, oZ + c * 1024, trfrag((b0 ^ (256 * (c >> 1))) + (c & 1) * 2048, (b0 ^ (256 * (c >> 1)) ^ 64) + (c & 1) * 2048)); SEND
    }
    SB();
    sfor<Z1_SPLIT, 56>(z1_step);
    bar();
    SFOR(c, 0, 6) vZ[c / 3][c % 3] = WS_Z + (g14 ^ (c < 3 ? 1 : 0)) * 16 * 16384 + w * 4096 + l * 16 + (c % 3) * 1024 - 2048; SEND
    SFOR(c, 0, 8) rz[c] = rot[c] * 64 + 2048 - (c & 1) * 2048; spin(rz[c]); SEND
    vprobe = l < 16 ? WS_Z + ((g14 ^ 1) * 16 + 8 * kh + vpos) * 16384 + w * 4096 + 4092 - 1024 * vrow8 : IDLE;
    const float ri = __builtin_amdgcn_rcpf(rs);
    nops<12>(z[0], z[1]);
    SFOR(i, 0, 16)
      if constexpr (i == Z_SPEC) ld32<true>(pr1, vprobe, sWS, zero);
      BG(T_W5 + i);
      z1v[i] = gelu2s(f32x2{z[i >> 3][(2 * i) & 15], z[i >> 3][((2 * i) & 15) + 1]}, k1, k3, ri);
      if constexpr (i & 1) SB();
    SEND
    waitpin<younger(K_ZP, 0, (T_W5 + 16) * 512)>(pr1);
  }
  u32x4 yb[28];
  auto yld = LAM(auto t_) { constexpr int t = CV(t_), k = t < 14 ? 2 * t : 2 * (t - 14) + 1; ld128<(k & 3) * 1024, true>(yb[t], vy, sWS, (k >> 2) * 4096); };
  {
    const float b = bflo(bbits);
    for (u32 redo = 0;;) {
      LANE_VARS(lane_id());
      u32 aZ = L_ZR + l * 16, aZw = aZ + mw[1], vG, vpr2, vfy, oG[4], oY[4];
      pin(aZ); pin(aZw);
      auto addr = LAM(auto n_) {
        constexpr int n = CV(n_), q = n & 3;
        if constexpr (n == 0) oY[0] = WS_Y + ((8 * P + 2 * w + blk) * 28 + 2 * g14) * 1024 + hf * 256 + t16 * 16;
        if constexpr (n == 4) oG[0] = WS_G + ((P * 14 + g14) * 4 + w) * 4096 + l * 16;
        if constexpr (n < 4) { oY[q] = oY[0] + q * 512; pin(oY[q]); }
        else if constexpr (n < 8) { oG[q] = oG[0] + q * 1024; pin(oG[q]); }
        else if constexpr (n < 12) { vO[q] = vrow8 * 896 + (vpos ^ ((blk | (2 * (q & 1))) << 1)) * 16 + 128 * j + q * (8 * 896 - 1024); pin(vO[q]); }
        else if constexpr (n == 12) {
          vpr2 = l < 8 ? WS_Z + (g14 * 16 + 8 * kh + l) * 16384 + w * 4096 + 4088 : (l < 10 ? WS_FLG + (2 * P + l - 8) * 128 : IDLE);
          pin(vpr2);
        }
        else if constexpr (n == 13) { vG = WS_G + ((P * 14 + (g14 ^ 1)) * 4 + w) * 4096 + l * 16; pin(vG); }
        else if constexpr (n == 14) {
          vfy = l < 2 ? WS_FLY + 128 * (2 * P + l) : IDLE;
          pin(vfy);
        } else { vy = WS_Y + (4 * p1 + w) * 28 * 1024 + l * 16; pin(vy); }
      };
      u32x4 zv[4];
      u32 zm;
      auto z_read = LAM(auto st_) { SFOR(q, 0, 4) zv[q] = lds128(aZw + CV(st_) * 16384 + q * 1024); SEND };
      auto z_max = LAM(auto h_) {
        constexpr int h = CV(h_);
        zm = umax3(umax3(h ? zm : zv[0].w, zv[2 * h].x, zv[2 * h].y), umax3(zv[2 * h].z, zv[2 * h + 1].x, zv[2 * h + 1].y), umax3(zv[2 * h].w, zv[2 * h + 1].z, zv[2 * h + 1].w));
        pin(zm);
      };
      auto z_again = LAM(auto st_) {
        do {
          SFOR(t, 2 * CV(st_), 2 * CV(st_) + 2) BG(T_W5 + Z_SPEC + t); SEND
          waitvm<0>();
          z_read(st_); z_max(IC(0)); z_max(IC(1));
        } while (__any(zm == SENT) && !giveup(redo, ws, tag));
      };
      z_read(IC(0)); z_max(IC(0)); z_max(IC(1));
      if (RARE(__any(zm == SENT))) z_again(IC(0));
      f32x16 acc[2];
      u32x4 fa[4], gp[2];
      float zg[16], chk = 0.f;
      u32x2 yf;
      u32x2 pr2;
      bool bad1, late;
      u32 gm;
      auto g_out = LAM(auto p_, auto u_) {
        constexpr int p = CV(p_), u = CV(u_);
        if constexpr (u == 0) pin(acc[p]);
        gp[u >> 2][u & 3] = cvtpk(acc[p][2 * u], acc[p][2 * u + 1]);
        chk = __builtin_fmaf(acc[p][2 * u], acc[p][2 * u + 1], chk);
        pin(chk);
        if constexpr ((u & 3) == 3) st128(sWS, oG[2 * p + (u >> 2)], gp[u >> 2]);
      };
      auto g_in = LAM(auto p_, auto u_) {
        constexpr int p = CV(p_), u = CV(u_);
        const u32 x = gp[u >> 3][(u >> 1) & 3];
        zg[u] = (((u & 1) ? bfhi(x) : bflo(x)) + b) * z1v[8 * p + (u >> 1)][u & 1];
        pin(zg[u]);
      };
      u32x4 yv;
      auto y_val = LAM(auto p_, auto u_) {
        constexpr int p = CV(p_), u = CV(u_);
        if constexpr (u == 0) pin(acc[p]);
        zg[2 * u] = __builtin_fmaf(acc[p][2 * u], z1v[8 * p + u].x, zg[2 * u]);
        zg[2 * u + 1] = __builtin_fmaf(acc[p][2 * u + 1], z1v[8 * p + u].y, zg[2 * u + 1]);
        if constexpr (p == 0) { pin(zg[2 * u]); pin(zg[2 * u + 1]); }
      };
      auto y_out = LAM(auto p_, auto u_) {
        constexpr int p = CV(p_), u = CV(u_);
        yv[u & 3] = cvtpk(zg[2 * u], zg[2 * u + 1]);
        chk = __builtin_fmaf(zg[2 * u], zg[2 * u + 1], chk);
        if constexpr (p == 0) pin(chk);
        if constexpr ((u & 3) == 3) st128(sWS, oY[2 * p + (u >> 2)], yv);
      };
      pipe<256, 4>(LAM(auto i_) {
        constexpr int i = CV(i_);
        if constexpr ((i & 15) == 0) {
          waitvm<younger(K_ZP, i >> 4, (T_P3 + i) * 512)>();
          rawbar();
        }
        BG(T_P3 + i);
        BGM(T_P3 + i + 1);
        fa[i & 3] = lds128(aZ + ((i >> 4) & 3) * 16384 + ((i >> 1) & 3) * 4096 + ((i >> 3) & 1) * 2048 + (i & 1) * 1024);
      }, LAM(auto i_) {
        constexpr int i = CV(i_);
        if constexpr ((i & 63) == 0) mm32az<0>(acc[(i >> 6) & 1], fa[i & 3]);
        else mm32a<4 * (i & 63)>(acc[(i >> 6) & 1], fa[i & 3]);
        if constexpr (i == Y_ASK) ld64<0, true>(yf, vfy, sWS, zero);
        if constexpr (i == Y_CK) waitpin<younger(K_OTH, 15, (T_P3 + i + 4) * 512)>(yf);
        if constexpr (i == Y_CK + 1) late = unset(yf, 3);
        if constexpr (i == Y_CK + 2) {
          if (RARE(late)) {
            do { ld64<0, true>(yf, vfy, sWS, zero); waitpin<0>(yf); } while (unset(yf, 3) && !giveup(redo, ws, tag));
          }
        }
        SFOR(st, 1, 3)
          constexpr int i0 = 16 * st - 10;
          if constexpr (i == i0) { waitvm<younger(K_ZP, st, (T_P3 + i + 4) * 512)>(); z_read(IC(st)); }
          if constexpr (i == i0 + 2 || i == i0 + 3) z_max(IC(i - i0 - 2));
          if constexpr (i == i0 + 4) late = lanes_eq(zm, SENT) != 0;
          if constexpr (i == i0 + 5) { if (RARE(late)) z_again(IC(st)); }
        SEND
        if constexpr (i == 1) late = lanes_eq(pr1, SENT) != 0;
        if constexpr (i == 2) {
          if (RARE(late)) { do { ld32<true>(pr1, vprobe, sWS, zero); waitpin<0>(pr1); } while (__any(pr1 == SENT) && !giveup(redo, ws, tag)); }
        }
        if constexpr (i >= 98 && i < 102) addr(IC(i - 98));
        if constexpr (i == 30) addr(IC(12));
        if constexpr (i >= 40 && i < 56 && !(i & 1)) addr(IC(4 + (i - 40) / 2));
        if constexpr (i >= 102 && i < 105) addr(IC(13 + i - 102));
        auto z_late = LAM() { return ((lanes_eq(pr2.y, SENT) & 0xff) | (lanes_ne(pr2, tag) & 0x300)) != 0; };
        if constexpr (i == Z_PR2) ld64<0, true>(pr2, vpr2, sWS, zero);
        if constexpr (i == Z_CK2) waitpin<younger(K_OTH, 4, (T_P3 + Z_CK2 + 4) * 512)>(pr2);
        if constexpr (i == Z_CK2 + 1) late = z_late();
        if constexpr (i == Z_CK2 + 2) {
          if (RARE(late)) { do { ld64<0, true>(pr2, vpr2, sWS, zero); waitpin<0>(pr2); } while (z_late() && !giveup(redo, ws, tag)); }
        }
        SFOR(p, 0, 2)
          if constexpr (i >= G_OUT[p] && i < G_OUT[p] + 8) g_out(IC(p), IC(i - G_OUT[p]));
          if constexpr (i == G_OUT[1] + 9 && p == 1) bad1 = __any(isnanr(chk));
          if constexpr (i >= G_IN[p] && i < G_IN[p] + 2) ld128<(2 * p + i - G_IN[p]) * 1024, true>(gp[i - G_IN[p]], vG, sWS, zero);
          auto g_max = LAM() { pin(gp[1]); gm = umax3(umax3(gp[0].x, gp[0].y, gp[0].z), umax3(gp[0].w, gp[1].x, gp[1].y), max(gp[1].z, gp[1].w)); pin(gm); };
          if constexpr (i == G_USE[p]) { waitpin<younger(K_OTH, 6 + p, (T_P3 + i + 4) * 512)>(gp[0]); g_max(); }
          if constexpr (i == G_USE[p] + 1) late = lanes_eq(gm, SENT) != 0;
          if constexpr (i == G_USE[p] + 2) {
            if (RARE(late) && !bad1) {
              do { SFOR(c, 0, 2) ld128<(2 * p + c) * 1024, true>(gp[c], vG, sWS, zero); SEND waitpin<0>(gp[0]); g_max(); } while (__any(gm == SENT) && !giveup(redo, ws, tag));
            }
          }
          if constexpr (i > G_USE[p] + 2 && i <= G_USE[p] + 18) g_in(IC(p), IC(i - G_USE[p] - 3));
        SEND
        if constexpr (i > Y_OUT && i <= Y_OUT + 4) { y_out(IC(0), IC(2 * (i - Y_OUT - 1))); y_out(IC(0), IC(2 * (i - Y_OUT - 1) + 1)); }
        if constexpr (i >= Y_OUT && i < Y_OUT + 4) { y_val(IC(0), IC(2 * (i - Y_OUT))); y_val(IC(0), IC(2 * (i - Y_OUT) + 1)); }
        if constexpr (i >= Y_IN && i < Y_IN + 2 * Y_PRE && !((i - Y_IN) & 1)) yld(IC((i - Y_IN) / 2));
      });
      nops<12>(acc[1]);
      SFOR(u, 0, 8) y_val(IC(1), IC(u)); y_out(IC(1), IC(u)); SEND
      waitvm<2>();
      SFOR(t, 0, Y_PRE) pin(yb[t]); SEND
      *(LAS u32*)(L_ZR + 16384 + 4 * w) = __any(isnanr(chk)) && !giveup(redo, ws, tag);
      bar();
      const u32x4 votes = lds128(L_ZR + 16384);
      if (!rfl(votes.x | votes.y | votes.z | votes.w)) break;
      waitvm<0>();
      bar();
      SFOR(c, 14, 16)
        constexpr int s = c - 12;
        setm0<sp_slot(s)>(mw[s < 2 ? 2 : 3]);
        SFOR(q, 0, 4) dman<q * 1024>(vS[c & 1][q], sWSP, rot[c >> 1]); SEND
      SEND
      SFOR(t, 0, 6) BG(T_W5 + Z_SPEC + t); SEND
      waitvm<0>();
    }
  }
  {
    LANE_VARS(lane_id());
    u32 aW[3][4];
    {
      const u32 xs = (trq >> 1) | ((g4 & 1) << 1);
      SFOR(n, 0, 4)
        aW[2][n] = (8 * g4 + trq) * 128 + ((n ^ xs) * 32) + 8 * trp + L_WOUT2;
        aW[0][n] = aW[2][n] + L_WOUT;
        aW[1][n] = aW[2][n] + L_WOUT + 65536;
        pin(aW[0][n]); pin(aW[1][n]); pin(aW[2][n]);
      SEND
    }
    f32x4 o4[4];
    for (u32 redo = 0;;) {
      u32x4 fa[3][2];
      SFOR(n, 0, 4) o4[n] = hacc[n]; SEND
      auto ald = LAM(auto i_) {
        constexpr int i = CV(i_), t = i >> 1, k = t < 14 ? 2 * t : 2 * (t - 14) + 1, x = k < 16 ? 0 : (k < 24 ? 1 : 2), o = (k & (k < 16 ? 15 : 7)) * 4096;
        SFOR(n, 0, 2) fa[i % 3][n] = trfrag(aW[x][2 * (i & 1) + n] + o, aW[x][2 * (i & 1) + n] + o + 512); SEND
      };
      sfor<Y_PRE, 14>(yld);
      sfor<0, 3>(ald);
      auto have = LAM(auto a_, auto b_, auto c_) {
        constexpr int a = CV(a_), b = CV(b_), c = CV(c_);
        auto bad = LAM() {
          u32 m = 0;
          SFOR(t, a, b) pin(yb[t]); m = max(m, yb[t].w); SEND
          return lanes_eq(m, SENT) != 0;
        };
        if (bad()) {
          waitvm<0>();
          SFOR(t, b, c) pin(yb[t]); SEND
          do {
            SFOR(t, a, c) if (lanes_eq(yb[t].w, SENT) != 0) yld(IC(t)); SEND
            waitvm<0>();
            SFOR(t, b, c) pin(yb[t]); SEND
          } while (bad() && !giveup(redo, ws, tag));
        }
      };
      auto steps = LAM(auto a_, auto b_) {
        SB();
        SFOR(i, 2 * CV(a_), 2 * CV(b_))
          mm16x2(o4[2 * (i & 1)], o4[2 * (i & 1) + 1], fa[i % 3][0], fa[i % 3][1], yb[i >> 1]);
          if constexpr (i + 3 < 56) ald(IC(i + 3));
          SB();
        SEND
      };
      have(IC(0), IC(Y_PRE), IC(14));
      nops4<2>(o4);
      steps(IC(0), IC(Y_PRE));
      waitvm<0>();
      have(IC(Y_PRE), IC(14), IC(14));
      sfor<14, 28>(yld);
      steps(IC(Y_PRE), IC(14));
      waitvm<7>();
      have(IC(14), IC(21), IC(28));
      steps(IC(14), IC(21));
      waitvm<0>();
      have(IC(21), IC(28), IC(28));
      steps(IC(21), IC(28));
      nops4<8>(o4);
      if (!__any(isnanr(o4[0][0])) || giveup(redo, ws, tag)) break;
      sfor<0, Y_PRE>(yld);
      waitvm<0>();
      SFOR(t, 0, Y_PRE) pin(yb[t]); SEND
    }
    const u32x4 sO4 = make_srd(out_, 2048 * 896);
    const u32 o = (64 * p1 + 16 * w + t16) * 896 + 128 * j + (g4 & 1) * 32 + (g4 >> 1) * 16;
    SFOR(m, 0, 2)
      const auto r0 = __builtin_amdgcn_permlane16_swap(cvtpk(o4[2 * m][0], o4[2 * m][1]), cvtpk(o4[2 * m + 1][0], o4[2 * m + 1][1]), false, false);
      const auto r1 = __builtin_amdgcn_permlane16_swap(cvtpk(o4[2 * m][2], o4[2 * m][3]), cvtpk(o4[2 * m + 1][2], o4[2 * m + 1][3]), false, false);
      raw_st128(u32x4{(u32)r0[0], (u32)r1[0], (u32)r0[1], (u32)r1[1]}, sO4, o + m * 64, 0, 16);
    SEND
  }
  asm volatile("fused_code_end:");
}

}

extern "C" void kernel_launch(void* const* d_in, const int* in_sizes, int n_in, void* d_out, int out_size, void* d_ws, size_t ws_size, hipStream_t stream) {
  fused_kernel<<<256, 256, 0, stream>>>(d_in[0], d_in[1], d_in[2], d_in[4], d_in[5], d_in[6], d_in[7], d_out, (char*)d_ws);
}
```
